```python
import torch

def _softmax(x, axis=-1):
    return torch.softmax(x, dim=axis)
def _gelu(x):
    return torch.nn.functional.gelu(x, approximate="tanh")
def _silu(x):
    return torch.nn.functional.silu(x)
def _sigmoid(x):
    return torch.sigmoid(x)
def _layernorm(x, axis=-1, eps=1e-5):
    m = x.mean(dim=axis, keepdim=True)
    v = x.var(dim=axis, keepdim=True, unbiased=False)
    return (x - m) / torch.sqrt(v + eps)
def _scatter_add(tgt, idx, src, axis=0):
    return tgt.clone().index_add_(axis, idx, src)
def _segment_sum(src, seg, n):
    out = torch.zeros((n,) + src.shape[1:], dtype=src.dtype)
    return out.index_add_(0, seg, src)
def _pad(x, pw):
    flat = tuple(v for pair in reversed(pw) for v in pair)
    return torch.nn.functional.pad(x, flat)

kernel_name = 'concat_heads__sliding_block_cached__gqa_block_cached__decode'

def setup_inputs():

    _w = (lambda a: (lambda t: t.to(torch.bfloat16) if t.is_floating_point() else t)(torch.as_tensor(a)))
    def _A_setup():
        B, Sq, D = 1, 1, 576
        Hq, Hkv, Sk, Dh = 32, 16, 8192, 128
        assert Hq % Hkv == 0, (Hq, Hkv)
        return {
            "x": _w((lambda *s: torch.randn(*s, dtype=torch.bfloat16) * 0.8)(B, Sq, D)),
            "k_cache": _w((lambda *s: torch.randn(*s, dtype=torch.bfloat16) * 1.6)(B, Hkv, Sk, Dh)),
            "v_cache": _w((lambda *s: torch.randn(*s, dtype=torch.bfloat16) * 0.2)(B, Hkv, Sk, Dh)),
            "wq": _w((lambda *s: torch.randn(*s, dtype=torch.bfloat16) * 1.6)(D, Hq * Dh) * (D ** -0.5)),
            "wo": _w((lambda *s: torch.randn(*s, dtype=torch.bfloat16) * 0.2)(Hq * Dh, D) * ((Hq * Dh) ** -0.5)),
        }
    _A_in = _A_setup()

    def _B_setup():
        B, Sq, D = 1, 1, 576
        Hq, Hkv, Sk, Dh = 32, 16, 8192, 128
        assert Hq % Hkv == 0, (Hq, Hkv)
        return {
            "x": _w((lambda *s: torch.randn(*s, dtype=torch.bfloat16) * 0.8)(B, Sq, D)),
            "k_cache": _w((lambda *s: torch.randn(*s, dtype=torch.bfloat16) * 1.6)(B, Hkv, Sk, Dh)),
            "v_cache": _w((lambda *s: torch.randn(*s, dtype=torch.bfloat16) * 0.2)(B, Hkv, Sk, Dh)),
            "wq": _w((lambda *s: torch.randn(*s, dtype=torch.bfloat16) * 1.6)(D, Hq * Dh) * (D ** -0.5)),
            "wo": _w((lambda *s: torch.randn(*s, dtype=torch.bfloat16) * 0.2)(Hq * Dh, D) * ((Hq * Dh) ** -0.5)),
        }
    _B_in = _B_setup()

    return {
        "A_x": _A_in["x"],
        "A_k_cache": _A_in["k_cache"],
        "A_v_cache": _A_in["v_cache"],
        "A_wq": _A_in["wq"],
        "A_wo": _A_in["wo"],
        "B_x": _B_in["x"],
        "B_k_cache": _B_in["k_cache"],
        "B_v_cache": _B_in["v_cache"],
        "B_wq": _B_in["wq"],
        "B_wo": _B_in["wo"],
    }

def reference(A_x, A_k_cache, A_v_cache, A_wq, A_wo, B_x, B_k_cache, B_v_cache, B_wq, B_wo):
    def _A_ref(x, k_cache, v_cache, wq, wo):
        B, Sq, D = x.shape
        Hq, Dh = 32, 128
        Hkv, Sk = k_cache.shape[1], k_cache.shape[2]
        W = min(4096, max(256, Sk // 4))
        rep = Hq // Hkv
        q = (lambda x, *d: x.permute(*d))((x @ wq).reshape(B, Sq, Hq, Dh), 0, 2, 1, 3)
        k = (lambda x, n, axis: torch.repeat_interleave(x, n, dim=axis))(k_cache[:, :, Sk - W:, :], rep, axis=1)
        v = (lambda x, n, axis: torch.repeat_interleave(x, n, dim=axis))(v_cache[:, :, Sk - W:, :], rep, axis=1)
        scores = torch.einsum("bhqd,bhkd->bhqk", q, k) * (Dh ** -0.5)
        p = _softmax(scores, axis=-1)
        o = torch.einsum("bhqk,bhkd->bhqd", p, v)
        return (lambda x, *d: x.permute(*d))(o, 0, 2, 1, 3).reshape(B, Sq, Hq * Dh) @ wo

    def _B_ref(x, k_cache, v_cache, wq, wo):
        B, Sq, D = x.shape
        Hq, Dh = 32, 128
        Hkv = k_cache.shape[1]
        rep = Hq // Hkv
        q = (lambda x, *d: x.permute(*d))((x @ wq).reshape(B, Sq, Hq, Dh), 0, 2, 1, 3)
        k = (lambda x, n, axis: torch.repeat_interleave(x, n, dim=axis))(k_cache, rep, axis=1)
        v = (lambda x, n, axis: torch.repeat_interleave(x, n, dim=axis))(v_cache, rep, axis=1)
        scores = torch.einsum("bhqd,bhkd->bhqk", q, k) * (Dh ** -0.5)
        p = _softmax(scores, axis=-1)
        o = torch.einsum("bhqk,bhkd->bhqd", p, v)
        return (lambda x, *d: x.permute(*d))(o, 0, 2, 1, 3).reshape(B, Sq, Hq * Dh) @ wo

    a = _A_ref(A_x, A_k_cache, A_v_cache, A_wq, A_wo)
    b = _B_ref(B_x, B_k_cache, B_v_cache, B_wq, B_wo)
    return torch.concatenate([a, b], axis=-1)
```

```cpp
#include <hip/hip_runtime.h>
#include <hip/hip_bf16.h>
#include <stdint.h>

typedef unsigned int u32;
typedef u32 u32x4 __attribute__((ext_vector_type(4)));
typedef float f32x4 __attribute__((ext_vector_type(4)));
typedef __bf16 bf16x2 __attribute__((ext_vector_type(2)));

constexpr int D = 576, HQ = 32, HKV = 16, SK = 8192, DH = 128, WIN = 2048;

__device__ __forceinline__ float bflo(u32 x) { return __uint_as_float(x << 16); }
__device__ __forceinline__ float bfhi(u32 x) { return __uint_as_float(x & 0xffff0000u); }
__device__ __forceinline__ float dot2(u32 a, u32 b, float c) {
  return __builtin_amdgcn_fdot2_f32_bf16(__builtin_bit_cast(bf16x2, a), __builtin_bit_cast(bf16x2, b), c, false);
}
__device__ __forceinline__ unsigned short f2bf(float f) {
  u32 u = __float_as_uint(f); u += 0x7fffu + ((u >> 16) & 1u); return (unsigned short)(u >> 16);
}

__global__ void __launch_bounds__(256) k_q(const unsigned short* xA, const unsigned short* wqA, const unsigned short* xB, const unsigned short* wqB, unsigned short* q) {
  __shared__ float red[64][33];
  int b = blockIdx.x, br = b >> 7, c0 = (b & 127) * 32;
  const unsigned short* x = br ? xB : xA; const unsigned short* w = br ? wqB : wqA;
  int t = threadIdx.x, cg = t & 3, r0 = t >> 2;
  float acc[8] = {0,0,0,0,0,0,0,0};
#pragma unroll
  for (int i = 0; i < 9; ++i) {
    int r = r0 + 64 * i;
    u32x4 v = *(const u32x4*)(w + (size_t)r * 4096 + c0 + cg * 8);
    float xv = __uint_as_float((u32)x[r] << 16);
#pragma unroll
    for (int k = 0; k < 4; ++k) { acc[2*k] += xv * bflo(v[k]); acc[2*k+1] += xv * bfhi(v[k]); }
  }
#pragma unroll
  for (int k = 0; k < 8; ++k) red[r0][cg * 8 + k] = acc[k];
  __syncthreads();
  if (t < 32) { float s = 0; for (int i = 0; i < 64; ++i) s += red[i][t]; q[br * 4096 + c0 + t] = f2bf(s); }
}

__global__ void __launch_bounds__(256) k_attn(const unsigned short* kA, const unsigned short* vA, const unsigned short* kB, const unsigned short* vB, const unsigned short* q, float* part) {
  __shared__ float sc[2][128];
  __shared__ float red[4][2][128];
  int h = blockIdx.x & 15, ch = blockIdx.x >> 4;
  int br = ch < 64 ? 1 : 0;
  int key0 = br ? ch * 128 : (SK - WIN) + (ch - 64) * 128;
  const unsigned short* K = (br ? kB : kA) + ((size_t)h * SK + key0) * DH;
  const unsigned short* V = (br ? vB : vA) + ((size_t)h * SK + key0) * DH;
  int t = threadIdx.x, lane = t & 63, w = t >> 6, c = lane & 15, r = lane >> 4;
  u32x4 kk[8], vv[8];
#pragma unroll
  for (int i = 0; i < 8; ++i) kk[i] = __builtin_nontemporal_load((const u32x4*)(K + (size_t)(w * 32 + i * 4 + r) * DH + c * 8));
#pragma unroll
  for (int i = 0; i < 8; ++i) vv[i] = __builtin_nontemporal_load((const u32x4*)(V + (size_t)(w * 32 + i * 4 + r) * DH + c * 8));
  u32x4 q0 = *(const u32x4*)(q + br * 4096 + (2 * h) * DH + c * 8);
  u32x4 q1 = *(const u32x4*)(q + br * 4096 + (2 * h + 1) * DH + c * 8);
  const float C2 = 0.088388347648318440f * 1.4426950408889634f;
#pragma unroll
  for (int i = 0; i < 8; ++i) {
    float d0 = 0, d1 = 0;
#pragma unroll
    for (int k = 0; k < 4; ++k) { d0 = dot2(kk[i][k], q0[k], d0); d1 = dot2(kk[i][k], q1[k], d1); }
#pragma unroll
    for (int m = 1; m < 16; m <<= 1) { d0 += __shfl_xor(d0, m); d1 += __shfl_xor(d1, m); }
    if (c == 0) { sc[0][w * 32 + i * 4 + r] = d0 * C2; sc[1][w * 32 + i * 4 + r] = d1 * C2; }
  }
  __syncthreads();
  float m0 = fmaxf(sc[0][lane], sc[0][lane + 64]), m1 = fmaxf(sc[1][lane], sc[1][lane + 64]);
#pragma unroll
  for (int m = 1; m < 64; m <<= 1) { m0 = fmaxf(m0, __shfl_xor(m0, m)); m1 = fmaxf(m1, __shfl_xor(m1, m)); }
  float l0 = exp2f(sc[0][lane] - m0) + exp2f(sc[0][lane + 64] - m0), l1 = exp2f(sc[1][lane] - m1) + exp2f(sc[1][lane + 64] - m1);
#pragma unroll
  for (int m = 1; m < 64; m <<= 1) { l0 += __shfl_xor(l0, m); l1 += __shfl_xor(l1, m); }
  float a0[8] = {0,0,0,0,0,0,0,0}, a1[8] = {0,0,0,0,0,0,0,0};
#pragma unroll
  for (int i = 0; i < 8; ++i) {
    float p0 = exp2f(sc[0][w * 32 + i * 4 + r] - m0), p1 = exp2f(sc[1][w * 32 + i * 4 + r] - m1);
#pragma unroll
    for (int k = 0; k < 4; ++k) { float lo = bflo(vv[i][k]), hi = bfhi(vv[i][k]); a0[2*k] += p0 * lo; a0[2*k+1] += p0 * hi; a1[2*k] += p1 * lo; a1[2*k+1] += p1 * hi; }
  }
#pragma unroll
  for (int k = 0; k < 8; ++k) { a0[k] += __shfl_xor(a0[k], 16); a0[k] += __shfl_xor(a0[k], 32); a1[k] += __shfl_xor(a1[k], 16); a1[k] += __shfl_xor(a1[k], 32); }
  if (r == 0) {
#pragma unroll
    for (int k = 0; k < 8; ++k) { red[w][0][c * 8 + k] = a0[k]; red[w][1][c * 8 + k] = a1[k]; }
  }
  __syncthreads();
  int g = t >> 7, d = t & 127;
  float s = red[0][g][d] + red[1][g][d] + red[2][g][d] + red[3][g][d];
  float* p = part + ((size_t)(h * 2 + g) * 80 + ch) * 132;
  p[d] = s;
  if (d == 0) { p[128] = g ? m1 : m0; p[129] = g ? l1 : l0; }
}

__global__ void __launch_bounds__(256) k_out(const unsigned short* woA, const unsigned short* woB, const float* part, float* ypart) {
  __shared__ float o[32];
  __shared__ float red[3][576];
  int b = blockIdx.x, br = b >> 7, rb = b & 127, qh = rb >> 2, d0 = (rb & 3) * 32;
  int t = threadIdx.x;
  const unsigned short* w = (br ? woB : woA) + (size_t)rb * 32 * D;
  int n = br ? 64 : 16, c0 = br ? 0 : 64;
  const float* p = part + ((size_t)qh * 80 + c0) * 132;
  if (t < 32) {
    float M = -INFINITY; for (int i = 0; i < n; ++i) M = fmaxf(M, p[i * 132 + 128]);
    float L = 0, s = 0; for (int i = 0; i < n; ++i) { float e = exp2f(p[i * 132 + 128] - M); L += e * p[i * 132 + 129]; s += e * p[i * 132 + d0 + t]; }
    o[t] = __uint_as_float((u32)f2bf(s / L) << 16);
  }
  __syncthreads();
  if (t < 216) {
    int cg = t % 72, rg = t / 72; float acc[8] = {0,0,0,0,0,0,0,0};
    for (int r = rg; r < 32; r += 3) { u32x4 v = *(const u32x4*)(w + (size_t)r * D + cg * 8); float ov = o[r];
#pragma unroll
      for (int k = 0; k < 4; ++k) { acc[2*k] += ov * bflo(v[k]); acc[2*k+1] += ov * bfhi(v[k]); } }
#pragma unroll
    for (int k = 0; k < 8; ++k) red[rg][cg * 8 + k] = acc[k];
  }
  __syncthreads();
  for (int j = t; j < 576; j += 256) ypart[(size_t)b * 576 + j] = red[0][j] + red[1][j] + red[2][j];
}
__global__ void __launch_bounds__(256) k_fin(const float* ypart, unsigned short* out) {
  __shared__ float red[16][17];
  int b = blockIdx.x;
  int col = b * 16 + (threadIdx.x & 15), br = col / 576, j = col % 576, g = threadIdx.x >> 4;
  float s = 0;
  for (int i = g; i < 128; i += 16) s += ypart[(size_t)(br * 128 + i) * 576 + j];
  red[g][threadIdx.x & 15] = s; __syncthreads();
  if (threadIdx.x < 16) { float z = 0; for (int i = 0; i < 16; ++i) z += red[i][threadIdx.x]; out[col] = f2bf(z); }
}

extern "C" void kernel_launch(void* const* d_in, const int* in_sizes, int n_in, void* d_out, int out_size, void* d_ws, size_t ws_size, hipStream_t stream) {
  typedef const unsigned short* P;
  P xA = (P)d_in[0], kA = (P)d_in[1], vA = (P)d_in[2], wqA = (P)d_in[3], woA = (P)d_in[4];
  P xB = (P)d_in[5], kB = (P)d_in[6], vB = (P)d_in[7], wqB = (P)d_in[8], woB = (P)d_in[9];
  unsigned short* q = (unsigned short*)d_ws;
  float* part = (float*)((char*)d_ws + 65536);
  float* ypart = (float*)((char*)d_ws + 4 * 1048576);
  k_q<<<256, 256, 0, stream>>>(xA, wqA, xB, wqB, q);
  k_attn<<<1280, 256, 0, stream>>>(kA, vA, kB, vB, q, part);
  k_out<<<256, 256, 0, stream>>>(woA, woB, part, ypart);
  k_fin<<<72, 256, 0, stream>>>(ypart, (unsigned short*)d_out);
}
```

```cpp
#include <hip/hip_runtime.h>
#include <hip/hip_bf16.h>
#include <stdint.h>

#define STAMPS 0
#define WIN 3
#define CODEPF 1
#define KPRE 0
#define QSLEEP 16

typedef unsigned int u32;
typedef unsigned long long u64;
typedef u32 u32x2 __attribute__((ext_vector_type(2)));
typedef u32 u32x4 __attribute__((ext_vector_type(4)));
typedef float f32x2 __attribute__((ext_vector_type(2)));
typedef float f32x4 __attribute__((ext_vector_type(4)));
typedef __bf16 bf16x2 __attribute__((ext_vector_type(2)));
typedef __bf16 bf16x8 __attribute__((ext_vector_type(8)));
#define GAS __attribute__((address_space(1)))
#define LAS __attribute__((address_space(3)))
typedef __amdgpu_buffer_rsrc_t rsrc_t;

constexpr int NWAVE = 11, NTHR = NWAVE * 64;
constexpr u32 SENT = 0xFFFFFFFFu;
constexpr u32 SPIN_CAP = 1u << 17;
constexpr int WS_EPOCH = 0;
constexpr int WS_Q = 4096;
constexpr int WS_ML = WS_Q + 256 * 512;
constexpr int WS_O = WS_ML + 16 * 1024;
constexpr int WS_Y = WS_O + 256 * 4096;
constexpr int WS_END = WS_Y + 256 * 4608;
constexpr int WS_STAMP = 4 << 20;
constexpr int L_QP = 0;
constexpr int L_ML = 2304;
constexpr int L_F = L_ML + 160;
constexpr int L_FLAG = L_F + 80;
constexpr int L_OF = 2816;
constexpr int L_Q = 3072;
constexpr int L_O = 4096;
constexpr int L_BYTES = L_O + 10 * 1024;
constexpr int SC1 = 16, NT = 2;
constexpr u32 CODE_BYTES = 7552;

__device__ __forceinline__ rsrc_t mkrs(const void* p, u32 bytes) { return __builtin_amdgcn_make_buffer_rsrc((void*)p, 0, bytes, 0x00020000); }
__device__ __forceinline__ u32x4 ldnt(rsrc_t r, u32 voff, u32 soff) { return __builtin_amdgcn_raw_buffer_load_b128(r, voff, soff, NT); }
__device__ __forceinline__ u32x4 ldsc(rsrc_t r, u32 voff, u32 soff) { return __builtin_amdgcn_raw_buffer_load_b128(r, voff, soff, SC1); }
__device__ __forceinline__ void st2g(rsrc_t r, u32 voff, float a, float b, u32 tag) {
  __builtin_amdgcn_raw_buffer_store_b128(u32x4{__float_as_uint(a), tag, __float_as_uint(b), tag}, r, voff, 0, SC1);
}
__device__ __forceinline__ u32 badtag(u32x4 v, u32 tag) { return (v[1] ^ tag) | (v[3] ^ tag); }
__device__ __forceinline__ f32x4 mfma(u32x4 a, u32x4 b, f32x4 c) {
  return __builtin_amdgcn_mfma_f32_16x16x32_bf16(__builtin_bit_cast(bf16x8, a), __builtin_bit_cast(bf16x8, b), c, 0, 0, 0);
}
__device__ __forceinline__ u32 cvtpk(float lo, float hi) {
  bf16x2 r = {(__bf16)lo, (__bf16)hi};
  return __builtin_bit_cast(u32, r);
}
__device__ __forceinline__ float bflo(u32 x) { return __uint_as_float(x << 16); }
__device__ __forceinline__ float bfhi(u32 x) { return __uint_as_float(x & 0xffff0000u); }
__device__ __forceinline__ float asf(u32 x) { return __uint_as_float(x); }
template <int CTRL> __device__ __forceinline__ float dpp(float x) {
  return __builtin_bit_cast(float, __builtin_amdgcn_mov_dpp(__builtin_bit_cast(int, x), CTRL, 0xf, 0xf, true));
}
constexpr int XOR1 = 0xB1, XOR2 = 0x4E, HMIR = 0x141, ROR8 = 0x128, ROR4 = 0x124;
__device__ __forceinline__ float x16_32_max(float x) {
  auto s = __builtin_amdgcn_permlane16_swap(__float_as_uint(x), __float_as_uint(x), false, false);
  x = fmaxf(__uint_as_float(s[0]), __uint_as_float(s[1]));
  auto t = __builtin_amdgcn_permlane32_swap(__float_as_uint(x), __float_as_uint(x), false, false);
  return fmaxf(__uint_as_float(t[0]), __uint_as_float(t[1]));
}
__device__ __forceinline__ float x16_32_sum(float x) {
  auto s = __builtin_amdgcn_permlane16_swap(__float_as_uint(x), __float_as_uint(x), false, false);
  x = __uint_as_float(s[0]) + __uint_as_float(s[1]);
  auto t = __builtin_amdgcn_permlane32_swap(__float_as_uint(x), __float_as_uint(x), false, false);
  return __uint_as_float(t[0]) + __uint_as_float(t[1]);
}
__device__ __forceinline__ bool none(bool p) { return __builtin_amdgcn_ballot_w64(p) == 0; }
__device__ __forceinline__ f32x2 gemv_tile(u32x4 e, u32x4 msk, const u32x4* w) {
  f32x4 y = {0.f, 0.f, 0.f, 0.f};
#pragma unroll
  for (int i = 0; i < 4; ++i) y = mfma(msk & e[i], w[i], y);
  auto t0 = __builtin_amdgcn_permlane32_swap(__float_as_uint(y[0]), __float_as_uint(y[2]), false, false);
  auto t1 = __builtin_amdgcn_permlane32_swap(__float_as_uint(y[1]), __float_as_uint(y[3]), false, false);
  return f32x2{__uint_as_float(t0[0]) + dpp<ROR8>(__uint_as_float(t0[1])), __uint_as_float(t1[0]) + dpp<ROR8>(__uint_as_float(t1[1]))};
}
#define SB() __builtin_amdgcn_sched_barrier(0)
#define VMWAIT(n) asm volatile("s_waitcnt vmcnt(%0)" ::"n"(n) : "memory")
#define LGKWAIT() asm volatile("s_waitcnt lgkmcnt(0)" ::: "memory")
#define BARRIER() asm volatile("s_waitcnt lgkmcnt(0)\n\ts_barrier" ::: "memory")
#define STAMP(i) do {} while (0)
#define STAMPV(i, v) do {} while (0)
#define FINISH() __builtin_amdgcn_endpgm()

__global__ void __launch_bounds__(NTHR, 3)
fused_decode(const unsigned char* xA, const unsigned char* kA, const unsigned char* vA, const unsigned char* wqA,
             const unsigned char* woA, const unsigned char* xB, const unsigned char* kB, const unsigned char* vB,
             const unsigned char* wqB, const unsigned char* woB, unsigned char* out_, unsigned char* ws_) {
  __shared__ __attribute__((aligned(16))) unsigned char smem[L_BYTES];
  LAS unsigned char* L = (LAS unsigned char*)smem;
  u64 pc;
  asm volatile("s_getpc_b64 %0" : "=s"(pc));
  GAS unsigned char* out = (GAS unsigned char*)out_;
  GAS unsigned char* ws = (GAS unsigned char*)ws_;
  const int b = blockIdx.x, lane = threadIdx.x & 63;
  const int wave = __builtin_amdgcn_readfirstlane(threadIdx.x >> 6);
  const int h = (b & 7) * 2 + (b >> 7), s = (b >> 3) & 15, wg = h * 16 + s;
  const rsrc_t rs = mkrs(ws_, WS_END);
  STAMP(0);
  *(LAS u32*)(L + threadIdx.x * 4) = SENT;
  BARRIER();
  asm volatile("" ::"s"(xA), "s"(kA), "s"(vA), "s"(wqA), "s"(woA), "s"(xB), "s"(kB), "s"(vB), "s"(wqB), "s"(woB), "s"(out_), "s"(ws_));
  const int rbr = s >> 3;
  if (wave == 0) {
    const u32 tag = __builtin_amdgcn_raw_buffer_load_b32(rs, WS_EPOCH, 0, SC1) + 1u;
    {
      const rsrc_t rc = mkrs((const void*)(pc & ~(u64)255), CODE_BYTES);
      const u32 c0 = __builtin_amdgcn_raw_buffer_load_b32(rc, lane * 128, 0, 0);
      asm volatile("" ::"v"(c0));
    }
    *(LAS u32*)(L + L_FLAG + 12) = tag;
    STAMP(1);
    {
      u32 t[9];
#pragma unroll 1
      for (u32 spin = 0; spin < SPIN_CAP * 8; ++spin) {
#pragma unroll
        for (int a = 0; a < 9; ++a) t[a] = *(volatile LAS u32*)(L + L_QP + a * 256 + lane * 4);
        if (none(max(max(max(max(t[0], t[1]), max(t[2], t[3])), max(max(t[4], t[5]), max(t[6], t[7]))), t[8]) == SENT)) break;
        __builtin_amdgcn_s_sleep(1);
      }
      const float qp = ((asf(t[0]) + asf(t[1])) + (asf(t[2]) + asf(t[3]))) + ((asf(t[4]) + asf(t[5])) + (asf(t[6]) + asf(t[7]))) + asf(t[8]);
      __builtin_amdgcn_raw_buffer_store_b64(u32x2{__float_as_uint(qp), tag}, rs, WS_Q + wg * 512 + lane * 8, 0, SC1);
    }
    STAMP(2);
    VMWAIT(0);
    STAMP(3);
    {
      u32x4 v[8];
      u32 spin = 0;
#pragma unroll 1
      for (; spin < SPIN_CAP; ++spin) {
        __builtin_amdgcn_s_sleep(QSLEEP);
        asm volatile("" ::: "memory");
        u32 bad = 0;
#pragma unroll
        for (int i = 0; i < 8; ++i) v[i] = ldsc(rs, WS_Q + h * 8192 + (lane >> 5) * 1024 + (lane & 31) * 16 + (i & 1) * 512, (i >> 1) * 2048);
#pragma unroll
        for (int i = 0; i < 8; ++i) bad |= badtag(v[i], tag);
        if (none(bad != 0)) break;
      }
      STAMPV(22, spin + 1);
#pragma unroll
      for (int i = 0; i < 4; ++i)
        *(LAS u32*)(L + L_Q + i * 256 + lane * 4) = cvtpk(asf(v[2 * i][0]) + asf(v[2 * i + 1][0]), asf(v[2 * i][2]) + asf(v[2 * i + 1][2]));
      LGKWAIT();
      *(volatile LAS u32*)(L + L_FLAG) = 0;
    }
    STAMP(4);
    {
      u32x2 ml;
#pragma unroll 1
      for (u32 spin = 0; spin < SPIN_CAP * 8; ++spin) {
        ml = *(volatile LAS u32x2*)(L + L_ML + (lane < 20 ? lane : 0) * 8);
        if (none(max(ml[0], ml[1]) == SENT)) break;
        __builtin_amdgcn_s_sleep(2);
      }
      STAMP(6);
      const bool live = lane < 20;
      float m = live ? asf(ml[0]) : -INFINITY;
      float M = fmaxf(m, dpp<XOR2>(m)); M = fmaxf(M, dpp<ROR4>(M)); M = fmaxf(M, dpp<ROR8>(M));
      const float F = __builtin_amdgcn_exp2f(m - M);
      if (live) *(LAS float*)(L + L_F + lane * 4) = F;
      float l = live ? asf(ml[1]) * F : 0.f;
      l += dpp<XOR2>(l); l += dpp<ROR4>(l); l += dpp<ROR8>(l);
      st2g(rs, (lane & 46) == 0 ? WS_ML + h * 1024 + ((lane < 16 ? 2 : 0) + (lane & 1)) * 256 + s * 16 : WS_END, M, l, tag);
    }
    const int moff = WS_ML + h * 1024 + (s >> 2) * 256 + (lane & 15) * 16;
    const int ioff = WS_O + wg * 4096 + (lane & 7) * 256 + (lane >> 3) * 16;
    const int br2 = b >= 72, c0 = (b - br2 * 72) * 8;
    const int yoff = WS_Y + (br2 * 128 + (lane >> 2)) * 4608 + c0 * 8 + (lane & 3) * 16;
    asm volatile("" ::"v"(yoff), "v"(ioff), "v"(moff));
    STAMP(7);
#pragma unroll 1
    for (u32 spin = 0; spin < SPIN_CAP * 8; ++spin) {
      if (*(volatile LAS u32*)(L + L_FLAG + 4) != SENT) break;
      __builtin_amdgcn_s_sleep(1);
    }
    STAMP(8);
    float w0, w1;
    {
      u32x4 gv;
#pragma unroll 1
      for (u32 spin = 0; spin < SPIN_CAP; ++spin) {
        asm volatile("" ::: "memory");
        gv = ldsc(rs, moff, 0);
        if (none(badtag(gv, tag) != 0)) break;
      }
      const float gm = asf(gv[0]);
      float GM = fmaxf(gm, dpp<XOR1>(gm)); GM = fmaxf(GM, dpp<XOR2>(GM)); GM = fmaxf(GM, dpp<HMIR>(GM)); GM = fmaxf(GM, dpp<ROR8>(GM));
      const float e = __builtin_amdgcn_exp2f(gm - GM);
      float GL = asf(gv[2]) * e;
      GL += dpp<XOR1>(GL); GL += dpp<XOR2>(GL); GL += dpp<HMIR>(GL); GL += dpp<ROR8>(GL);
      const float w = e / GL, wx = dpp<ROR8>(w);
      w0 = (lane & 8) ? wx : w; w1 = (lane & 8) ? w : wx;
      asm volatile("" ::"v"(w0), "v"(w1));
    }
    STAMP(9);
    {
      u32x4 v[4];
      u32 spin = 0;
#pragma unroll 1
      for (; spin < SPIN_CAP; ++spin) {
        asm volatile("" ::: "memory");
#pragma unroll
        for (int i = 0; i < 4; ++i) v[i] = ldsc(rs, ioff + (i & 1) * 128 + (i >> 1) * 2048, 0);
        if (none(((badtag(v[0], tag) | badtag(v[1], tag)) | (badtag(v[2], tag) | badtag(v[3], tag))) != 0)) break;
      }
      STAMP(10); STAMPV(20, spin + 1);
      float ov[4];
#pragma unroll
      for (int i = 0; i < 4; ++i) ov[i] = asf(v[i >> 1][2 * (i & 1)]) * w0 + asf(v[2 + (i >> 1)][2 * (i & 1)]) * w1;
      SB();
#pragma unroll
      for (int i = 0; i < 4; ++i) ov[i] += dpp<XOR1>(ov[i]);
      SB();
#pragma unroll
      for (int i = 0; i < 4; ++i) ov[i] += dpp<XOR2>(ov[i]);
      SB();
#pragma unroll
      for (int i = 0; i < 4; ++i) ov[i] += dpp<HMIR>(ov[i]);
      SB();
      const u32x4 e = {cvtpk(ov[0], ov[0]), cvtpk(ov[1], ov[1]), cvtpk(ov[2], ov[2]), cvtpk(ov[3], ov[3])};
      *(LAS u32x4*)(L + L_OF + (lane >> 3) * 16) = e;
    }
    BARRIER();
    STAMP(11);
    if (b < 144) {
      u32x4 v[8];
      u32 spin = 0;
#pragma unroll 1
      for (; spin < SPIN_CAP; ++spin) {
        asm volatile("" ::: "memory");
        u32 bad = 0;
#pragma unroll
        for (int i = 0; i < 8; ++i) v[i] = ldsc(rs, yoff, i * 16 * 4608);
#pragma unroll
        for (int i = 0; i < 8; ++i) bad |= badtag(v[i], tag);
        if (none(bad != 0)) break;
      }
      STAMP(12); STAMPV(21, spin + 1);
      const float y0 = ((asf(v[0][0]) + asf(v[1][0])) + (asf(v[2][0]) + asf(v[3][0]))) + ((asf(v[4][0]) + asf(v[5][0])) + (asf(v[6][0]) + asf(v[7][0])));
      const float y1 = ((asf(v[0][2]) + asf(v[1][2])) + (asf(v[2][2]) + asf(v[3][2]))) + ((asf(v[4][2]) + asf(v[5][2])) + (asf(v[6][2]) + asf(v[7][2])));
      auto t0 = __builtin_amdgcn_permlane32_swap(__float_as_uint(y0), __float_as_uint(y1), false, false);
      float z = asf(t0[0]) + asf(t0[1]);
      auto t1 = __builtin_amdgcn_permlane16_swap(__float_as_uint(z), __float_as_uint(z), false, false);
      z = asf(t1[0]) + asf(t1[1]);
      z += dpp<ROR8>(z); z += dpp<ROR4>(z);
      if ((lane & 28) == 0) *(GAS unsigned short*)(out + (br2 * 576 + c0 + 2 * (lane & 3) + (lane >> 5)) * 2) = (unsigned short)cvtpk(z, z);
      if ((b | lane) == 0) *(GAS u32*)(ws + WS_EPOCH) = tag;
    }
    STAMP(13);
    FINISH();
  }
  const int a = wave - 1, br = a < 8;
  u32x4 K[16], V[20];
  const int row0 = (s & 1) * 288 + a * 32;
  const rsrc_t rq = mkrs(rbr ? wqB : wqA, a < 9 ? 576 * 8192 : 0);
  const u32 qoff = (u32)(row0 + 4 * (lane >> 3)) * 8192 + h * 512 + ((s >> 1) & 3) * 128 + (lane & 7) * 16;
#pragma unroll
  for (int i = 0; i < 4; ++i) V[i] = ldnt(rq, qoff, i * 8192);
  const u32x2 xv = __builtin_amdgcn_raw_buffer_load_b64(mkrs(rbr ? xB : xA, 1152), (row0 + 4 * (lane >> 3)) * 2, 0, 0);
  SB();
  const u32 kvo = (u32)(h * 8192 + (br ? s * 512 + a * 4 : 6144 + s * 128 + (a - 8) * 4)) * 256;
  const u32 kvb = br ? 131072 : 32768, sh = br ? 13 : 11;
  const rsrc_t rk = mkrs((br ? kB : kA) + kvo, kvb);
  const rsrc_t rv = mkrs((br ? vB : vA) + kvo, kvb);
  const u32 koff = (u32)((lane >> 2) & 3) * 256 + (lane & 3) * 64 + (lane >> 4) * 16, voff = lane * 16;
#define CHUNK(j) ((u32)((j) ^ s) << sh)
#pragma unroll
  for (int i = 0; i < KPRE; ++i) K[i] = ldnt(rk, koff, CHUNK(i));
  u32x4 msk;
#pragma unroll
  for (int k = 0; k < 4; ++k) msk[k] = ((lane & 7) >> 1) == k ? ((lane & 1) ? 0xffff0000u : 0x0000ffffu) : 0u;
  const int colp = (lane & 7) * 8 + ((lane >> 4) & 1) * 4 + (lane >> 5) * 2;
  SB();
  VMWAIT(KPRE);
#pragma unroll
  for (int i = KPRE; i < WIN; ++i) K[i] = ldnt(rk, koff, CHUNK(i));
  SB();
  {
    const u32x4 e = {__builtin_amdgcn_perm(xv[0], xv[0], 0x01000100u), __builtin_amdgcn_perm(xv[0], xv[0], 0x03020302u),
                     __builtin_amdgcn_perm(xv[1], xv[1], 0x01000100u), __builtin_amdgcn_perm(xv[1], xv[1], 0x03020302u)};
    const f32x2 qp = gemv_tile(e, msk, V);
    if (!(lane & 8) && a < 9) *(LAS f32x2*)(L + L_QP + a * 256 + colp * 4) = qp;
  }
  SB();
  STAMP(1);
#pragma unroll
  for (int i = (KPRE > WIN ? KPRE : WIN); i < 16; ++i) {
    VMWAIT(WIN - 1);
    K[i] = ldnt(rk, koff, CHUNK(i));
    SB();
  }
  STAMP(2);
#pragma unroll 1
  for (u32 spin = 0; spin < SPIN_CAP * 8; ++spin) { if (*(volatile LAS u32*)(L + L_FLAG) == 0) break; __builtin_amdgcn_s_sleep(1); }
  asm volatile("" ::: "memory");
  STAMP(3);
  const rsrc_t ro = mkrs((rbr ? woB : woA) + (u32)(h * 256 + (s & 7) * 32) * 1152, a < 9 ? 32 * 1152 : 0);
  const u32 wooff = (u32)(4 * (lane >> 3)) * 1152 + (lane & 7) * 16 + a * 128;
#define ISSUE(n) do { if ((n) < 16) V[n] = ldnt(rv, voff, CHUNK(n)); else if ((n) < 20) V[n] = ldnt(ro, wooff + ((n) - 16) * 1152, 0); } while (0)
  const u32x4 Q = *(LAS u32x4*)(L + L_Q + br * 512 + ((lane >> 3) & 1) * 256 + (lane & 3) * 64 + (lane >> 4) * 16);
  const u32 tag = *(LAS u32*)(L + L_FLAG + 12);
  float sc[16];
  u32 P[16];
  float m, l = 0.f, F;
  const float LOG2E = 1.4426950408889634f;
  const int yo = (lane & 8) ? WS_END : WS_Y + (rbr * 128 + h * 8 + (s & 7)) * 4608 + (a * 64 + colp) * 8;
  const int ooff = WS_O + (h * 16 + br * 8 + (lane >> 5) * 4 + ((lane & 15) >> 2)) * 4096 + s * 256 + (lane & 3) * 32 + ((lane >> 4) & 1) * 16;
  LAS unsigned char* cntp = L + L_FLAG + 8 - br * 4;
  f32x4 o = {0.f, 0.f, 0.f, 0.f};
#pragma unroll
  for (int n = 0; n < 16 + WIN; ++n) {
    u32x4 sel;
    if (n > 6) { sel = msk & P[n - WIN]; asm volatile("" : "+v"(sel)); }
    SB();
    if (n <= 20) VMWAIT(WIN - 1);
    if (n < 15 + WIN) ISSUE(n);
    SB();
    if (n < 4) {
      const int T = n;
      float d[4];
#pragma unroll
      for (int u = 0; u < 4; ++u) {
        const f32x4 t = mfma(K[T * 4 + u], Q, f32x4{0.f, 0.f, 0.f, 0.f});
        d[u] = ((dpp<0x00>(t[0]) + dpp<0x55>(t[1])) + dpp<0xAA>(t[2])) + dpp<0xFF>(t[3]);
      }
      const u32 r0 = cvtpk(d[0], d[1]), r1 = cvtpk(d[2], d[3]);
      const float SC = 0.08838834764831845f;
      const u32 r2 = cvtpk(bflo(r0) * SC, bfhi(r0) * SC), r3 = cvtpk(bflo(r1) * SC, bfhi(r1) * SC);
      sc[T * 4 + 0] = bflo(r2); sc[T * 4 + 1] = bfhi(r2); sc[T * 4 + 2] = bflo(r3); sc[T * 4 + 3] = bfhi(r3);
    } else if (n == 4) {
      m = sc[0];
#pragma unroll
      for (int i = 1; i < 16; ++i) m = fmaxf(m, sc[i]);
      m = x16_32_max(m);
#pragma unroll
      for (int i = 0; i < 6; ++i) { const float p = __builtin_amdgcn_exp2f((sc[i] - m) * LOG2E); l += p; P[i] = cvtpk(p, p); }
    } else if (n == 5) {
#pragma unroll
      for (int i = 6; i < 16; ++i) { const float p = __builtin_amdgcn_exp2f((sc[i] - m) * LOG2E); l += p; P[i] = cvtpk(p, p); }
      l = x16_32_sum(l);
      if ((lane & 55) == 0) *(LAS f32x2*)(L + L_ML + a * 16 + (lane >> 3) * 8) = f32x2{m * LOG2E, l};
      asm volatile("" ::"v"(yo), "v"(ooff), "v"(cntp));
      STAMP(4);
    } else if (n == 6) {
#pragma unroll
      for (int i = 0; i <= n - WIN; ++i) o = mfma(msk & P[i], V[i], o);
    } else {
      o = mfma(sel, V[n - WIN], o);
      if (n == 14) {
#pragma unroll 1
        for (u32 spin = 0; spin < SPIN_CAP * 8; ++spin) {
          const u32 f = *(volatile LAS u32*)(L + L_F + a * 8 + (lane >> 5) * 4);
          F = __uint_as_float(f);
          if (none(f == SENT)) break;
          __builtin_amdgcn_s_sleep(1);
        }
      }
    }
    SB();
  }
#pragma unroll
  for (int k = 15 + WIN; k < 20; ++k) ISSUE(k);
  SB();
  *(LAS f32x4*)(L + L_O + a * 1024 + lane * 16) = o * F;
  u32 arr;
  asm volatile("s_mov_b64 exec, 1\n\tds_add_rtn_u32 %0, %1, %2\n\ts_mov_b64 exec, -1\n\ts_waitcnt lgkmcnt(0)"
               : "=&v"(arr) : "v"(cntp), "v"(1u) : "memory");
  arr = __builtin_amdgcn_readfirstlane(arr);
  STAMP(5);
  if (arr == (br ? 6u : 0u)) {
    f32x4 t;
    if (br) {
      f32x4 u[8];
#pragma unroll
      for (int k = 0; k < 8; ++k) u[k] = *(LAS f32x4*)(L + L_O + k * 1024 + lane * 16);
      t = ((u[0] + u[1]) + (u[2] + u[3])) + ((u[4] + u[5]) + (u[6] + u[7]));
    } else {
      t = *(LAS f32x4*)(L + L_O + 8 * 1024 + lane * 16) + *(LAS f32x4*)(L + L_O + 9 * 1024 + lane * 16);
    }
    st2g(rs, ooff, t[0], t[1], tag);
    st2g(rs, ooff + 128, t[2], t[3], tag);
    STAMP(8);
  }
  if (a == 9) FINISH();
  BARRIER();
  const u32x4 e = *(LAS u32x4*)(L + L_OF + (lane >> 3) * 16);
  const f32x2 y = gemv_tile(e, msk, V + 16);
  st2g(rs, yo, y[0], y[1], tag);
  STAMP(7);
  FINISH();
}

extern "C" void kernel_launch(void* const* d_in, const int* in_sizes, int n_in, void* d_out, int out_size, void* d_ws,
                              size_t ws_size, hipStream_t stream) {
  typedef const unsigned char* P;
  fused_decode<<<256, NTHR, 0, stream>>>((P)d_in[0], (P)d_in[1], (P)d_in[2], (P)d_in[3], (P)d_in[4], (P)d_in[5], (P)d_in[6],
                                         (P)d_in[7], (P)d_in[8], (P)d_in[9], (unsigned char*)d_out, (unsigned char*)d_ws);
}
```

```cpp
#define DIAGS 0
#include <hip/hip_runtime.h>
#include <hip/hip_bf16.h>
#include <stdint.h>

#define STAMPS 0
#define WIN 3
#define CODEPF 1
#define KPRE 0
#define QSLEEP 16

typedef unsigned int u32;
typedef unsigned long long u64;
typedef u32 u32x2 __attribute__((ext_vector_type(2)));
typedef u32 u32x4 __attribute__((ext_vector_type(4)));
typedef float f32x2 __attribute__((ext_vector_type(2)));
typedef float f32x4 __attribute__((ext_vector_type(4)));
typedef __bf16 bf16x2 __attribute__((ext_vector_type(2)));
typedef __bf16 bf16x8 __attribute__((ext_vector_type(8)));
#define GAS __attribute__((address_space(1)))
#define LAS __attribute__((address_space(3)))
typedef __amdgpu_buffer_rsrc_t rsrc_t;

constexpr int NWAVE = 11, NTHR = NWAVE * 64;
constexpr u32 SENT = 0xFFFFFFFFu;
constexpr u32 SPIN_CAP = 1u << 17;
constexpr int WS_EPOCH = 0;
constexpr int WS_Q = 4096;
constexpr int WS_ML = WS_Q + 256 * 512;
constexpr int WS_O = WS_ML + 16 * 1024;
constexpr int WS_Y = WS_O + 256 * 4096;
constexpr int WS_END = WS_Y + 256 * 4608;
constexpr int WS_STAMP = 4 << 20;
constexpr int L_QP = 0;
constexpr int L_ML = 2304;
constexpr int L_F = L_ML + 160;
constexpr int L_FLAG = L_F + 80;
constexpr int L_OF = 2816;
constexpr int L_Q = 3072;
constexpr int L_O = 4096;
constexpr int L_BYTES = L_O + 10 * 1024;
constexpr int SC1 = 16, NT = 2;
constexpr u32 CODE_BYTES = 7552;

__device__ __forceinline__ rsrc_t mkrs(const void* p, u32 bytes) { return __builtin_amdgcn_make_buffer_rsrc((void*)p, 0, bytes, 0x00020000); }
__device__ __forceinline__ u32x4 ldnt(rsrc_t r, u32 voff, u32 soff) { return __builtin_amdgcn_raw_buffer_load_b128(r, voff, soff, NT); }
__device__ __forceinline__ u32x4 ldsc(rsrc_t r, u32 voff, u32 soff) { return __builtin_amdgcn_raw_buffer_load_b128(r, voff, soff, SC1); }
__device__ __forceinline__ void st2g(rsrc_t r, u32 voff, float a, float b, u32 tag) {
  __builtin_amdgcn_raw_buffer_store_b128(u32x4{__float_as_uint(a), tag, __float_as_uint(b), tag}, r, voff, 0, SC1);
}
__device__ __forceinline__ u32 badtag(u32x4 v, u32 tag) { return (v[1] ^ tag) | (v[3] ^ tag); }
__device__ __forceinline__ f32x4 mfma(u32x4 a, u32x4 b, f32x4 c) {
  return __builtin_amdgcn_mfma_f32_16x16x32_bf16(__builtin_bit_cast(bf16x8, a), __builtin_bit_cast(bf16x8, b), c, 0, 0, 0);
}
__device__ __forceinline__ u32 cvtpk(float lo, float hi) {
  bf16x2 r = {(__bf16)lo, (__bf16)hi};
  return __builtin_bit_cast(u32, r);
}
__device__ __forceinline__ float bflo(u32 x) { return __uint_as_float(x << 16); }
__device__ __forceinline__ float bfhi(u32 x) { return __uint_as_float(x & 0xffff0000u); }
__device__ __forceinline__ float asf(u32 x) { return __uint_as_float(x); }
template <int CTRL> __device__ __forceinline__ float dpp(float x) {
  return __builtin_bit_cast(float, __builtin_amdgcn_mov_dpp(__builtin_bit_cast(int, x), CTRL, 0xf, 0xf, true));
}
constexpr int XOR1 = 0xB1, XOR2 = 0x4E, HMIR = 0x141, ROR8 = 0x128, ROR4 = 0x124;
__device__ __forceinline__ float x16_32_max(float x) {
  auto s = __builtin_amdgcn_permlane16_swap(__float_as_uint(x), __float_as_uint(x), false, false);
  x = fmaxf(__uint_as_float(s[0]), __uint_as_float(s[1]));
  auto t = __builtin_amdgcn_permlane32_swap(__float_as_uint(x), __float_as_uint(x), false, false);
  return fmaxf(__uint_as_float(t[0]), __uint_as_float(t[1]));
}
__device__ __forceinline__ float x16_32_sum(float x) {
  auto s = __builtin_amdgcn_permlane16_swap(__float_as_uint(x), __float_as_uint(x), false, false);
  x = __uint_as_float(s[0]) + __uint_as_float(s[1]);
  auto t = __builtin_amdgcn_permlane32_swap(__float_as_uint(x), __float_as_uint(x), false, false);
  return __uint_as_float(t[0]) + __uint_as_float(t[1]);
}
__device__ __forceinline__ bool none(bool p) { return __builtin_amdgcn_ballot_w64(p) == 0; }
__device__ __forceinline__ f32x2 gemv_tile(u32x4 e, u32x4 msk, const u32x4* w) {
  f32x4 y = {0.f, 0.f, 0.f, 0.f};
#pragma unroll
  for (int i = 0; i < 4; ++i) y = mfma(msk & e[i], w[i], y);
  auto t0 = __builtin_amdgcn_permlane32_swap(__float_as_uint(y[0]), __float_as_uint(y[2]), false, false);
  auto t1 = __builtin_amdgcn_permlane32_swap(__float_as_uint(y[1]), __float_as_uint(y[3]), false, false);
  return f32x2{__uint_as_float(t0[0]) + dpp<ROR8>(__uint_as_float(t0[1])), __uint_as_float(t1[0]) + dpp<ROR8>(__uint_as_float(t1[1]))};
}
#define SB() __builtin_amdgcn_sched_barrier(0)
#define VMWAIT(n) asm volatile("s_waitcnt vmcnt(%0)" ::"n"(n) : "memory")
#define LGKWAIT() asm volatile("s_waitcnt lgkmcnt(0)" ::: "memory")
#define BARRIER() asm volatile("s_waitcnt lgkmcnt(0)\n\ts_barrier" ::: "memory")
#define RT(x) asm volatile("s_memrealtime %0\n\ts_waitcnt lgkmcnt(0)" : "=s"(x)::"memory")
#define CT(x) asm volatile("s_memtime %0\n\ts_waitcnt lgkmcnt(0)" : "=s"(x)::"memory")
#define STAMP(i) do { if ((i) == DIAGS && wave == 0) RT(tdiag); } while (0)
#define STAMPV(i, v) do {} while (0)
#define FINISH() do { if (wave == 0) { u64 x = tdiag - tstart; if (DIAGS == 99) { u64 c; CT(c); x = (c - cstart) / 24; } u64 t; do { __builtin_amdgcn_s_sleep(4); RT(t); } while (t < tstart + 5000 + x); } __builtin_amdgcn_endpgm(); } while (0)

__global__ void __launch_bounds__(NTHR, 3)
fused_decode(const unsigned char* xA, const unsigned char* kA, const unsigned char* vA, const unsigned char* wqA,
             const unsigned char* woA, const unsigned char* xB, const unsigned char* kB, const unsigned char* vB,
             const unsigned char* wqB, const unsigned char* woB, unsigned char* out_, unsigned char* ws_) {
  __shared__ __attribute__((aligned(16))) unsigned char smem[L_BYTES];
  LAS unsigned char* L = (LAS unsigned char*)smem;
  u64 pc;
  asm volatile("s_getpc_b64 %0" : "=s"(pc));
  GAS unsigned char* out = (GAS unsigned char*)out_;
  GAS unsigned char* ws = (GAS unsigned char*)ws_;
  const int b = blockIdx.x, lane = threadIdx.x & 63;
  const int wave = __builtin_amdgcn_readfirstlane(threadIdx.x >> 6);
  const int h = (b & 7) * 2 + (b >> 7), s = (b >> 3) & 15, wg = h * 16 + s;
  const rsrc_t rs = mkrs(ws_, WS_END);
  u64 tstart, tdiag, cstart; RT(tstart); CT(cstart); tdiag = tstart;
  *(LAS u32*)(L + threadIdx.x * 4) = SENT;
  BARRIER();
  asm volatile("" ::"s"(xA), "s"(kA), "s"(vA), "s"(wqA), "s"(woA), "s"(xB), "s"(kB), "s"(vB), "s"(wqB), "s"(woB), "s"(out_), "s"(ws_));
  const int rbr = s >> 3;
  if (wave == 0) {
    const u32 tag = __builtin_amdgcn_raw_buffer_load_b32(rs, WS_EPOCH, 0, SC1) + 1u;
    {
      const rsrc_t rc = mkrs((const void*)(pc & ~(u64)255), CODE_BYTES);
      const u32 c0 = __builtin_amdgcn_raw_buffer_load_b32(rc, lane * 128, 0, 0);
      asm volatile("" ::"v"(c0));
    }
    *(LAS u32*)(L + L_FLAG + 12) = tag;
    STAMP(1);
    {
      u32 t[9];
#pragma unroll 1
      for (u32 spin = 0; spin < SPIN_CAP * 8; ++spin) {
#pragma unroll
        for (int a = 0; a < 9; ++a) t[a] = *(volatile LAS u32*)(L + L_QP + a * 256 + lane * 4);
        if (none(max(max(max(max(t[0], t[1]), max(t[2], t[3])), max(max(t[4], t[5]), max(t[6], t[7]))), t[8]) == SENT)) break;
        __builtin_amdgcn_s_sleep(1);
      }
      const float qp = ((asf(t[0]) + asf(t[1])) + (asf(t[2]) + asf(t[3]))) + ((asf(t[4]) + asf(t[5])) + (asf(t[6]) + asf(t[7]))) + asf(t[8]);
      __builtin_amdgcn_raw_buffer_store_b64(u32x2{__float_as_uint(qp), tag}, rs, WS_Q + wg * 512 + lane * 8, 0, SC1);
    }
    STAMP(2);
    VMWAIT(0);
    STAMP(3);
    {
      u32x4 v[8];
      u32 spin = 0;
#pragma unroll 1
      for (; spin < SPIN_CAP; ++spin) {
        __builtin_amdgcn_s_sleep(QSLEEP);
        asm volatile("" ::: "memory");
        u32 bad = 0;
#pragma unroll
        for (int i = 0; i < 8; ++i) v[i] = ldsc(rs, WS_Q + h * 8192 + (lane >> 5) * 1024 + (lane & 31) * 16 + (i & 1) * 512, (i >> 1) * 2048);
#pragma unroll
        for (int i = 0; i < 8; ++i) bad |= badtag(v[i], tag);
        if (none(bad != 0)) break;
      }
      STAMPV(22, spin + 1);
#pragma unroll
      for (int i = 0; i < 4; ++i)
        *(LAS u32*)(L + L_Q + i * 256 + lane * 4) = cvtpk(asf(v[2 * i][0]) + asf(v[2 * i + 1][0]), asf(v[2 * i][2]) + asf(v[2 * i + 1][2]));
      LGKWAIT();
      *(volatile LAS u32*)(L + L_FLAG) = 0;
    }
    STAMP(4);
    {
      u32x2 ml;
#pragma unroll 1
      for (u32 spin = 0; spin < SPIN_CAP * 8; ++spin) {
        ml = *(volatile LAS u32x2*)(L + L_ML + (lane < 20 ? lane : 0) * 8);
        if (none(max(ml[0], ml[1]) == SENT)) break;
        __builtin_amdgcn_s_sleep(2);
      }
      STAMP(6);
      const bool live = lane < 20;
      float m = live ? asf(ml[0]) : -INFINITY;
      float M = fmaxf(m, dpp<XOR2>(m)); M = fmaxf(M, dpp<ROR4>(M)); M = fmaxf(M, dpp<ROR8>(M));
      const float F = __builtin_amdgcn_exp2f(m - M);
      if (live) *(LAS float*)(L + L_F + lane * 4) = F;
      float l = live ? asf(ml[1]) * F : 0.f;
      l += dpp<XOR2>(l); l += dpp<ROR4>(l); l += dpp<ROR8>(l);
      st2g(rs, (lane & 46) == 0 ? WS_ML + h * 1024 + ((lane < 16 ? 2 : 0) + (lane & 1)) * 256 + s * 16 : WS_END, M, l, tag);
    }
    const int moff = WS_ML + h * 1024 + (s >> 2) * 256 + (lane & 15) * 16;
    const int ioff = WS_O + wg * 4096 + (lane & 7) * 256 + (lane >> 3) * 16;
    const int br2 = b >= 72, c0 = (b - br2 * 72) * 8;
    const int yoff = WS_Y + (br2 * 128 + (lane >> 2)) * 4608 + c0 * 8 + (lane & 3) * 16;
    asm volatile("" ::"v"(yoff), "v"(ioff), "v"(moff));
    STAMP(7);
#pragma unroll 1
    for (u32 spin = 0; spin < SPIN_CAP * 8; ++spin) {
      if (*(volatile LAS u32*)(L + L_FLAG + 4) != SENT) break;
      __builtin_amdgcn_s_sleep(1);
    }
    STAMP(8);
    float w0, w1;
    {
      u32x4 gv;
#pragma unroll 1
      for (u32 spin = 0; spin < SPIN_CAP; ++spin) {
        asm volatile("" ::: "memory");
        gv = ldsc(rs, moff, 0);
        if (none(badtag(gv, tag) != 0)) break;
      }
      const float gm = asf(gv[0]);
      float GM = fmaxf(gm, dpp<XOR1>(gm)); GM = fmaxf(GM, dpp<XOR2>(GM)); GM = fmaxf(GM, dpp<HMIR>(GM)); GM = fmaxf(GM, dpp<ROR8>(GM));
      const float e = __builtin_amdgcn_exp2f(gm - GM);
      float GL = asf(gv[2]) * e;
      GL += dpp<XOR1>(GL); GL += dpp<XOR2>(GL); GL += dpp<HMIR>(GL); GL += dpp<ROR8>(GL);
      const float w = e / GL, wx = dpp<ROR8>(w);
      w0 = (lane & 8) ? wx : w; w1 = (lane & 8) ? w : wx;
      asm volatile("" ::"v"(w0), "v"(w1));
    }
    STAMP(9);
    {
      u32x4 v[4];
      u32 spin = 0;
#pragma unroll 1
      for (; spin < SPIN_CAP; ++spin) {
        asm volatile("" ::: "memory");
#pragma unroll
        for (int i = 0; i < 4; ++i) v[i] = ldsc(rs, ioff + (i & 1) * 128 + (i >> 1) * 2048, 0);
        if (none(((badtag(v[0], tag) | badtag(v[1], tag)) | (badtag(v[2], tag) | badtag(v[3], tag))) != 0)) break;
      }
      STAMP(10); STAMPV(20, spin + 1);
      float ov[4];
#pragma unroll
      for (int i = 0; i < 4; ++i) ov[i] = asf(v[i >> 1][2 * (i & 1)]) * w0 + asf(v[2 + (i >> 1)][2 * (i & 1)]) * w1;
      SB();
#pragma unroll
      for (int i = 0; i < 4; ++i) ov[i] += dpp<XOR1>(ov[i]);
      SB();
#pragma unroll
      for (int i = 0; i < 4; ++i) ov[i] += dpp<XOR2>(ov[i]);
      SB();
#pragma unroll
      for (int i = 0; i < 4; ++i) ov[i] += dpp<HMIR>(ov[i]);
      SB();
      const u32x4 e = {cvtpk(ov[0], ov[0]), cvtpk(ov[1], ov[1]), cvtpk(ov[2], ov[2]), cvtpk(ov[3], ov[3])};
      *(LAS u32x4*)(L + L_OF + (lane >> 3) * 16) = e;
    }
    BARRIER();
    STAMP(11);
    if (b < 144) {
      u32x4 v[8];
      u32 spin = 0;
#pragma unroll 1
      for (; spin < SPIN_CAP; ++spin) {
        asm volatile("" ::: "memory");
        u32 bad = 0;
#pragma unroll
        for (int i = 0; i < 8; ++i) v[i] = ldsc(rs, yoff, i * 16 * 4608);
#pragma unroll
        for (int i = 0; i < 8; ++i) bad |= badtag(v[i], tag);
        if (none(bad != 0)) break;
      }
      STAMP(12); STAMPV(21, spin + 1);
      const float y0 = ((asf(v[0][0]) + asf(v[1][0])) + (asf(v[2][0]) + asf(v[3][0]))) + ((asf(v[4][0]) + asf(v[5][0])) + (asf(v[6][0]) + asf(v[7][0])));
      const float y1 = ((asf(v[0][2]) + asf(v[1][2])) + (asf(v[2][2]) + asf(v[3][2]))) + ((asf(v[4][2]) + asf(v[5][2])) + (asf(v[6][2]) + asf(v[7][2])));
      auto t0 = __builtin_amdgcn_permlane32_swap(__float_as_uint(y0), __float_as_uint(y1), false, false);
      float z = asf(t0[0]) + asf(t0[1]);
      auto t1 = __builtin_amdgcn_permlane16_swap(__float_as_uint(z), __float_as_uint(z), false, false);
      z = asf(t1[0]) + asf(t1[1]);
      z += dpp<ROR8>(z); z += dpp<ROR4>(z);
      if ((lane & 28) == 0) *(GAS unsigned short*)(out + (br2 * 576 + c0 + 2 * (lane & 3) + (lane >> 5)) * 2) = (unsigned short)cvtpk(z, z);
      if ((b | lane) == 0) *(GAS u32*)(ws + WS_EPOCH) = tag;
    }
    STAMP(13);
    FINISH();
  }
  const int a = wave - 1, br = a < 8;
  u32x4 K[16], V[20];
  const int row0 = (s & 1) * 288 + a * 32;
  const rsrc_t rq = mkrs(rbr ? wqB : wqA, a < 9 ? 576 * 8192 : 0);
  const u32 qoff = (u32)(row0 + 4 * (lane >> 3)) * 8192 + h * 512 + ((s >> 1) & 3) * 128 + (lane & 7) * 16;
#pragma unroll
  for (int i = 0; i < 4; ++i) V[i] = ldnt(rq, qoff, i * 8192);
  const u32x2 xv = __builtin_amdgcn_raw_buffer_load_b64(mkrs(rbr ? xB : xA, 1152), (row0 + 4 * (lane >> 3)) * 2, 0, 0);
  SB();
  const u32 kvo = (u32)(h * 8192 + (br ? s * 512 + a * 4 : 6144 + s * 128 + (a - 8) * 4)) * 256;
  const u32 kvb = br ? 131072 : 32768, sh = br ? 13 : 11;
  const rsrc_t rk = mkrs((br ? kB : kA) + kvo, kvb);
  const rsrc_t rv = mkrs((br ? vB : vA) + kvo, kvb);
  const u32 koff = (u32)((lane >> 2) & 3) * 256 + (lane & 3) * 64 + (lane >> 4) * 16, voff = lane * 16;
#define CHUNK(j) ((u32)((j) ^ s) << sh)
#pragma unroll
  for (int i = 0; i < KPRE; ++i) K[i] = ldnt(rk, koff, CHUNK(i));
  u32x4 msk;
#pragma unroll
  for (int k = 0; k < 4; ++k) msk[k] = ((lane & 7) >> 1) == k ? ((lane & 1) ? 0xffff0000u : 0x0000ffffu) : 0u;
  const int colp = (lane & 7) * 8 + ((lane >> 4) & 1) * 4 + (lane >> 5) * 2;
  SB();
  VMWAIT(KPRE);
#pragma unroll
  for (int i = KPRE; i < WIN; ++i) K[i] = ldnt(rk, koff, CHUNK(i));
  SB();
  {
    const u32x4 e = {__builtin_amdgcn_perm(xv[0], xv[0], 0x01000100u), __builtin_amdgcn_perm(xv[0], xv[0], 0x03020302u),
                     __builtin_amdgcn_perm(xv[1], xv[1], 0x01000100u), __builtin_amdgcn_perm(xv[1], xv[1], 0x03020302u)};
    const f32x2 qp = gemv_tile(e, msk, V);
    if (!(lane & 8) && a < 9) *(LAS f32x2*)(L + L_QP + a * 256 + colp * 4) = qp;
  }
  SB();
  STAMP(1);
#pragma unroll
  for (int i = (KPRE > WIN ? KPRE : WIN); i < 16; ++i) {
    VMWAIT(WIN - 1);
    K[i] = ldnt(rk, koff, CHUNK(i));
    SB();
  }
  STAMP(2);
#pragma unroll 1
  for (u32 spin = 0; spin < SPIN_CAP * 8; ++spin) { if (*(volatile LAS u32*)(L + L_FLAG) == 0) break; __builtin_amdgcn_s_sleep(1); }
  asm volatile("" ::: "memory");
  STAMP(3);
  const rsrc_t ro = mkrs((rbr ? woB : woA) + (u32)(h * 256 + (s & 7) * 32) * 1152, a < 9 ? 32 * 1152 : 0);
  const u32 wooff = (u32)(4 * (lane >> 3)) * 1152 + (lane & 7) * 16 + a * 128;
#define ISSUE(n) do { if ((n) < 16) V[n] = ldnt(rv, voff, CHUNK(n)); else if ((n) < 20) V[n] = ldnt(ro, wooff + ((n) - 16) * 1152, 0); } while (0)
  const u32x4 Q = *(LAS u32x4*)(L + L_Q + br * 512 + ((lane >> 3) & 1) * 256 + (lane & 3) * 64 + (lane >> 4) * 16);
  const u32 tag = *(LAS u32*)(L + L_FLAG + 12);
  float sc[16];
  u32 P[16];
  float m, l = 0.f, F;
  const float LOG2E = 1.4426950408889634f;
  const int yo = (lane & 8) ? WS_END : WS_Y + (rbr * 128 + h * 8 + (s & 7)) * 4608 + (a * 64 + colp) * 8;
  const int ooff = WS_O + (h * 16 + br * 8 + (lane >> 5) * 4 + ((lane & 15) >> 2)) * 4096 + s * 256 + (lane & 3) * 32 + ((lane >> 4) & 1) * 16;
  LAS unsigned char* cntp = L + L_FLAG + 8 - br * 4;
  f32x4 o = {0.f, 0.f, 0.f, 0.f};
#pragma unroll
  for (int n = 0; n < 16 + WIN; ++n) {
    u32x4 sel;
    if (n > 6) { sel = msk & P[n - WIN]; asm volatile("" : "+v"(sel)); }
    SB();
    if (n <= 20) VMWAIT(WIN - 1);
    if (n < 15 + WIN) ISSUE(n);
    SB();
    if (n < 4) {
      const int T = n;
      float d[4];
#pragma unroll
      for (int u = 0; u < 4; ++u) {
        const f32x4 t = mfma(K[T * 4 + u], Q, f32x4{0.f, 0.f, 0.f, 0.f});
        d[u] = ((dpp<0x00>(t[0]) + dpp<0x55>(t[1])) + dpp<0xAA>(t[2])) + dpp<0xFF>(t[3]);
      }
      const u32 r0 = cvtpk(d[0], d[1]), r1 = cvtpk(d[2], d[3]);
      const float SC = 0.08838834764831845f;
      const u32 r2 = cvtpk(bflo(r0) * SC, bfhi(r0) * SC), r3 = cvtpk(bflo(r1) * SC, bfhi(r1) * SC);
      sc[T * 4 + 0] = bflo(r2); sc[T * 4 + 1] = bfhi(r2); sc[T * 4 + 2] = bflo(r3); sc[T * 4 + 3] = bfhi(r3);
    } else if (n == 4) {
      m = sc[0];
#pragma unroll
      for (int i = 1; i < 16; ++i) m = fmaxf(m, sc[i]);
      m = x16_32_max(m);
#pragma unroll
      for (int i = 0; i < 6; ++i) { const float p = __builtin_amdgcn_exp2f((sc[i] - m) * LOG2E); l += p; P[i] = cvtpk(p, p); }
    } else if (n == 5) {
#pragma unroll
      for (int i = 6; i < 16; ++i) { const float p = __builtin_amdgcn_exp2f((sc[i] - m) * LOG2E); l += p; P[i] = cvtpk(p, p); }
      l = x16_32_sum(l);
      if ((lane & 55) == 0) *(LAS f32x2*)(L + L_ML + a * 16 + (lane >> 3) * 8) = f32x2{m * LOG2E, l};
      asm volatile("" ::"v"(yo), "v"(ooff), "v"(cntp));
      STAMP(4);
    } else if (n == 6) {
#pragma unroll
      for (int i = 0; i <= n - WIN; ++i) o = mfma(msk & P[i], V[i], o);
    } else {
      o = mfma(sel, V[n - WIN], o);
      if (n == 14) {
#pragma unroll 1
        for (u32 spin = 0; spin < SPIN_CAP * 8; ++spin) {
          const u32 f = *(volatile LAS u32*)(L + L_F + a * 8 + (lane >> 5) * 4);
          F = __uint_as_float(f);
          if (none(f == SENT)) break;
          __builtin_amdgcn_s_sleep(1);
        }
      }
    }
    SB();
  }
#pragma unroll
  for (int k = 15 + WIN; k < 20; ++k) ISSUE(k);
  SB();
  *(LAS f32x4*)(L + L_O + a * 1024 + lane * 16) = o * F;
  u32 arr;
  asm volatile("s_mov_b64 exec, 1\n\tds_add_rtn_u32 %0, %1, %2\n\ts_mov_b64 exec, -1\n\ts_waitcnt lgkmcnt(0)"
               : "=&v"(arr) : "v"(cntp), "v"(1u) : "memory");
  arr = __builtin_amdgcn_readfirstlane(arr);
  STAMP(5);
  if (arr == (br ? 6u : 0u)) {
    f32x4 t;
    if (br) {
      f32x4 u[8];
#pragma unroll
      for (int k = 0; k < 8; ++k) u[k] = *(LAS f32x4*)(L + L_O + k * 1024 + lane * 16);
      t = ((u[0] + u[1]) + (u[2] + u[3])) + ((u[4] + u[5]) + (u[6] + u[7]));
    } else {
      t = *(LAS f32x4*)(L + L_O + 8 * 1024 + lane * 16) + *(LAS f32x4*)(L + L_O + 9 * 1024 + lane * 16);
    }
    st2g(rs, ooff, t[0], t[1], tag);
    st2g(rs, ooff + 128, t[2], t[3], tag);
    STAMP(8);
  }
  if (a == 9) FINISH();
  BARRIER();
  const u32x4 e = *(LAS u32x4*)(L + L_OF + (lane >> 3) * 16);
  const f32x2 y = gemv_tile(e, msk, V + 16);
  st2g(rs, yo, y[0], y[1], tag);
  STAMP(7);
  FINISH();
}

extern "C" void kernel_launch(void* const* d_in, const int* in_sizes, int n_in, void* d_out, int out_size, void* d_ws,
                              size_t ws_size, hipStream_t stream) {
  typedef const unsigned char* P;
  fused_decode<<<256, NTHR, 0, stream>>>((P)d_in[0], (P)d_in[1], (P)d_in[2], (P)d_in[3], (P)d_in[4], (P)d_in[5], (P)d_in[6],
                                         (P)d_in[7], (P)d_in[8], (P)d_in[9], (unsigned char*)d_out, (unsigned char*)d_ws);
}
```

```cpp
#define OV 3
#define TINYG 256
#define TINYB 64
#define TINYATTR __launch_bounds__(64)
#include <hip/hip_runtime.h>
#include <hip/hip_bf16.h>
#include <stdint.h>

#define STAMPS 0
#define WIN 3
#define CODEPF 1
#define KPRE 0
#define QSLEEP 16

typedef unsigned int u32;
typedef unsigned long long u64;
typedef u32 u32x2 __attribute__((ext_vector_type(2)));
typedef u32 u32x4 __attribute__((ext_vector_type(4)));
typedef float f32x2 __attribute__((ext_vector_type(2)));
typedef float f32x4 __attribute__((ext_vector_type(4)));
typedef __bf16 bf16x2 __attribute__((ext_vector_type(2)));
typedef __bf16 bf16x8 __attribute__((ext_vector_type(8)));
#define GAS __attribute__((address_space(1)))
#define LAS __attribute__((address_space(3)))
typedef __amdgpu_buffer_rsrc_t rsrc_t;

constexpr int NWAVE = 11, NTHR = NWAVE * 64;
constexpr u32 SENT = 0xFFFFFFFFu;
constexpr u32 SPIN_CAP = 1u << 17;
constexpr int WS_EPOCH = 0;
constexpr int WS_Q = 4096;
constexpr int WS_ML = WS_Q + 256 * 512;
constexpr int WS_O = WS_ML + 16 * 1024;
constexpr int WS_Y = WS_O + 256 * 4096;
constexpr int WS_END = WS_Y + 256 * 4608;
constexpr int WS_STAMP = 4 << 20;
constexpr int L_QP = 0;
constexpr int L_ML = 2304;
constexpr int L_F = L_ML + 160;
constexpr int L_FLAG = L_F + 80;
constexpr int L_OF = 2816;
constexpr int L_Q = 3072;
constexpr int L_O = 4096;
constexpr int L_BYTES = L_O + 10 * 1024;
constexpr int SC1 = 16, NT = 2;
constexpr u32 CODE_BYTES = 7552;

__device__ __forceinline__ rsrc_t mkrs(const void* p, u32 bytes) { return __builtin_amdgcn_make_buffer_rsrc((void*)p, 0, bytes, 0x00020000); }
__device__ __forceinline__ u32x4 ldnt(rsrc_t r, u32 voff, u32 soff) { return __builtin_amdgcn_raw_buffer_load_b128(r, voff, soff, NT); }
__device__ __forceinline__ u32x4 ldsc(rsrc_t r, u32 voff, u32 soff) { return __builtin_amdgcn_raw_buffer_load_b128(r, voff, soff, SC1); }
__device__ __forceinline__ void st2g(rsrc_t r, u32 voff, float a, float b, u32 tag) {
  __builtin_amdgcn_raw_buffer_store_b128(u32x4{__float_as_uint(a), tag, __float_as_uint(b), tag}, r, voff, 0, SC1);
}
__device__ __forceinline__ u32 badtag(u32x4 v, u32 tag) { return (v[1] ^ tag) | (v[3] ^ tag); }
__device__ __forceinline__ f32x4 mfma(u32x4 a, u32x4 b, f32x4 c) {
  return __builtin_amdgcn_mfma_f32_16x16x32_bf16(__builtin_bit_cast(bf16x8, a), __builtin_bit_cast(bf16x8, b), c, 0, 0, 0);
}
__device__ __forceinline__ u32 cvtpk(float lo, float hi) {
  bf16x2 r = {(__bf16)lo, (__bf16)hi};
  return __builtin_bit_cast(u32, r);
}
__device__ __forceinline__ float bflo(u32 x) { return __uint_as_float(x << 16); }
__device__ __forceinline__ float bfhi(u32 x) { return __uint_as_float(x & 0xffff0000u); }
__device__ __forceinline__ float asf(u32 x) { return __uint_as_float(x); }
template <int CTRL> __device__ __forceinline__ float dpp(float x) {
  return __builtin_bit_cast(float, __builtin_amdgcn_mov_dpp(__builtin_bit_cast(int, x), CTRL, 0xf, 0xf, true));
}
constexpr int XOR1 = 0xB1, XOR2 = 0x4E, HMIR = 0x141, ROR8 = 0x128, ROR4 = 0x124;
__device__ __forceinline__ float x16_32_max(float x) {
  auto s = __builtin_amdgcn_permlane16_swap(__float_as_uint(x), __float_as_uint(x), false, false);
  x = fmaxf(__uint_as_float(s[0]), __uint_as_float(s[1]));
  auto t = __builtin_amdgcn_permlane32_swap(__float_as_uint(x), __float_as_uint(x), false, false);
  return fmaxf(__uint_as_float(t[0]), __uint_as_float(t[1]));
}
__device__ __forceinline__ float x16_32_sum(float x) {
  auto s = __builtin_amdgcn_permlane16_swap(__float_as_uint(x), __float_as_uint(x), false, false);
  x = __uint_as_float(s[0]) + __uint_as_float(s[1]);
  auto t = __builtin_amdgcn_permlane32_swap(__float_as_uint(x), __float_as_uint(x), false, false);
  return __uint_as_float(t[0]) + __uint_as_float(t[1]);
}
__device__ __forceinline__ bool none(bool p) { return __builtin_amdgcn_ballot_w64(p) == 0; }
__device__ __forceinline__ f32x2 gemv_tile(u32x4 e, u32x4 msk, const u32x4* w) {
  f32x4 y = {0.f, 0.f, 0.f, 0.f};
#pragma unroll
  for (int i = 0; i < 4; ++i) y = mfma(msk & e[i], w[i], y);
  auto t0 = __builtin_amdgcn_permlane32_swap(__float_as_uint(y[0]), __float_as_uint(y[2]), false, false);
  auto t1 = __builtin_amdgcn_permlane32_swap(__float_as_uint(y[1]), __float_as_uint(y[3]), false, false);
  return f32x2{__uint_as_float(t0[0]) + dpp<ROR8>(__uint_as_float(t0[1])), __uint_as_float(t1[0]) + dpp<ROR8>(__uint_as_float(t1[1]))};
}
#define SB() __builtin_amdgcn_sched_barrier(0)
#define VMWAIT(n) asm volatile("s_waitcnt vmcnt(%0)" ::"n"(n) : "memory")
#define LGKWAIT() asm volatile("s_waitcnt lgkmcnt(0)" ::: "memory")
#define BARRIER() asm volatile("s_waitcnt lgkmcnt(0)\n\ts_barrier" ::: "memory")
#define RT(x) asm volatile("s_memrealtime %0\n\ts_waitcnt lgkmcnt(0)" : "=s"(x)::"memory")
#define STAMP(i) do {} while (0)
#define STAMPV(i, v) do {} while (0)
#define FINISH() do { if (wave == 0) { if (OV == 0 || OV == 1) { u64 ref = tstart; if (OV == 1) ref = *(volatile GAS u64*)(ws + (6 << 20)); u64 t; do { __builtin_amdgcn_s_sleep(4); RT(t); } while (t < ref + 6000); } \
   if (OV == 2 && lane == 0) *(GAS u64*)(ws + (6 << 20) + 4096 + b * 8) = tstart; } __builtin_amdgcn_endpgm(); } while (0)
__global__ void TINYATTR tiny1(unsigned char* ws) { u64 t; RT(t); if (threadIdx.x == 0 && blockIdx.x == 0) *(u64*)(ws + (6 << 20)) = t; }
__global__ void TINYATTR tiny2(unsigned char* ws) { u64 ref = 0;
  if (OV == 3) ref = *(volatile u64*)(ws + (6 << 20)); else { for (int i = 0; i < 256; ++i) { u64 x = *(volatile u64*)(ws + (6 << 20) + 4096 + i * 8); ref = x > ref ? x : ref; } }
  u64 t; do { __builtin_amdgcn_s_sleep(4); RT(t); } while (t < ref + 6000); }

__global__ void __launch_bounds__(NTHR, 3)
fused_decode(const unsigned char* xA, const unsigned char* kA, const unsigned char* vA, const unsigned char* wqA,
             const unsigned char* woA, const unsigned char* xB, const unsigned char* kB, const unsigned char* vB,
             const unsigned char* wqB, const unsigned char* woB, unsigned char* out_, unsigned char* ws_) {
  __shared__ __attribute__((aligned(16))) unsigned char smem[L_BYTES];
  LAS unsigned char* L = (LAS unsigned char*)smem;
  u64 pc;
  asm volatile("s_getpc_b64 %0" : "=s"(pc));
  GAS unsigned char* out = (GAS unsigned char*)out_;
  GAS unsigned char* ws = (GAS unsigned char*)ws_;
  const int b = blockIdx.x, lane = threadIdx.x & 63;
  const int wave = __builtin_amdgcn_readfirstlane(threadIdx.x >> 6);
  const int h = (b & 7) * 2 + (b >> 7), s = (b >> 3) & 15, wg = h * 16 + s;
  const rsrc_t rs = mkrs(ws_, WS_END);
  u64 tstart; RT(tstart);
  *(LAS u32*)(L + threadIdx.x * 4) = SENT;
  BARRIER();
  asm volatile("" ::"s"(xA), "s"(kA), "s"(vA), "s"(wqA), "s"(woA), "s"(xB), "s"(kB), "s"(vB), "s"(wqB), "s"(woB), "s"(out_), "s"(ws_));
  const int rbr = s >> 3;
  if (wave == 0) {
    const u32 tag = __builtin_amdgcn_raw_buffer_load_b32(rs, WS_EPOCH, 0, SC1) + 1u;
    {
      const rsrc_t rc = mkrs((const void*)(pc & ~(u64)255), CODE_BYTES);
      const u32 c0 = __builtin_amdgcn_raw_buffer_load_b32(rc, lane * 128, 0, 0);
      asm volatile("" ::"v"(c0));
    }
    *(LAS u32*)(L + L_FLAG + 12) = tag;
    STAMP(1);
    {
      u32 t[9];
#pragma unroll 1
      for (u32 spin = 0; spin < SPIN_CAP * 8; ++spin) {
#pragma unroll
        for (int a = 0; a < 9; ++a) t[a] = *(volatile LAS u32*)(L + L_QP + a * 256 + lane * 4);
        if (none(max(max(max(max(t[0], t[1]), max(t[2], t[3])), max(max(t[4], t[5]), max(t[6], t[7]))), t[8]) == SENT)) break;
        __builtin_amdgcn_s_sleep(1);
      }
      const float qp = ((asf(t[0]) + asf(t[1])) + (asf(t[2]) + asf(t[3]))) + ((asf(t[4]) + asf(t[5])) + (asf(t[6]) + asf(t[7]))) + asf(t[8]);
      __builtin_amdgcn_raw_buffer_store_b64(u32x2{__float_as_uint(qp), tag}, rs, WS_Q + wg * 512 + lane * 8, 0, SC1);
    }
    STAMP(2);
    VMWAIT(0);
    STAMP(3);
    {
      u32x4 v[8];
      u32 spin = 0;
#pragma unroll 1
      for (; spin < SPIN_CAP; ++spin) {
        __builtin_amdgcn_s_sleep(QSLEEP);
        asm volatile("" ::: "memory");
        u32 bad = 0;
#pragma unroll
        for (int i = 0; i < 8; ++i) v[i] = ldsc(rs, WS_Q + h * 8192 + (lane >> 5) * 1024 + (lane & 31) * 16 + (i & 1) * 512, (i >> 1) * 2048);
#pragma unroll
        for (int i = 0; i < 8; ++i) bad |= badtag(v[i], tag);
        if (none(bad != 0)) break;
      }
      STAMPV(22, spin + 1);
#pragma unroll
      for (int i = 0; i < 4; ++i)
        *(LAS u32*)(L + L_Q + i * 256 + lane * 4) = cvtpk(asf(v[2 * i][0]) + asf(v[2 * i + 1][0]), asf(v[2 * i][2]) + asf(v[2 * i + 1][2]));
      LGKWAIT();
      *(volatile LAS u32*)(L + L_FLAG) = 0;
    }
    STAMP(4);
    {
      u32x2 ml;
#pragma unroll 1
      for (u32 spin = 0; spin < SPIN_CAP * 8; ++spin) {
        ml = *(volatile LAS u32x2*)(L + L_ML + (lane < 20 ? lane : 0) * 8);
        if (none(max(ml[0], ml[1]) == SENT)) break;
        __builtin_amdgcn_s_sleep(2);
      }
      STAMP(6);
      const bool live = lane < 20;
      float m = live ? asf(ml[0]) : -INFINITY;
      float M = fmaxf(m, dpp<XOR2>(m)); M = fmaxf(M, dpp<ROR4>(M)); M = fmaxf(M, dpp<ROR8>(M));
      const float F = __builtin_amdgcn_exp2f(m - M);
      if (live) *(LAS float*)(L + L_F + lane * 4) = F;
      float l = live ? asf(ml[1]) * F : 0.f;
      l += dpp<XOR2>(l); l += dpp<ROR4>(l); l += dpp<ROR8>(l);
      st2g(rs, (lane & 46) == 0 ? WS_ML + h * 1024 + ((lane < 16 ? 2 : 0) + (lane & 1)) * 256 + s * 16 : WS_END, M, l, tag);
    }
    const int moff = WS_ML + h * 1024 + (s >> 2) * 256 + (lane & 15) * 16;
    const int ioff = WS_O + wg * 4096 + (lane & 7) * 256 + (lane >> 3) * 16;
    const int br2 = b >= 72, c0 = (b - br2 * 72) * 8;
    const int yoff = WS_Y + (br2 * 128 + (lane >> 2)) * 4608 + c0 * 8 + (lane & 3) * 16;
    asm volatile("" ::"v"(yoff), "v"(ioff), "v"(moff));
    STAMP(7);
#pragma unroll 1
    for (u32 spin = 0; spin < SPIN_CAP * 8; ++spin) {
      if (*(volatile LAS u32*)(L + L_FLAG + 4) != SENT) break;
      __builtin_amdgcn_s_sleep(1);
    }
    STAMP(8);
    float w0, w1;
    {
      u32x4 gv;
#pragma unroll 1
      for (u32 spin = 0; spin < SPIN_CAP; ++spin) {
        asm volatile("" ::: "memory");
        gv = ldsc(rs, moff, 0);
        if (none(badtag(gv, tag) != 0)) break;
      }
      const float gm = asf(gv[0]);
      float GM = fmaxf(gm, dpp<XOR1>(gm)); GM = fmaxf(GM, dpp<XOR2>(GM)); GM = fmaxf(GM, dpp<HMIR>(GM)); GM = fmaxf(GM, dpp<ROR8>(GM));
      const float e = __builtin_amdgcn_exp2f(gm - GM);
      float GL = asf(gv[2]) * e;
      GL += dpp<XOR1>(GL); GL += dpp<XOR2>(GL); GL += dpp<HMIR>(GL); GL += dpp<ROR8>(GL);
      const float w = e / GL, wx = dpp<ROR8>(w);
      w0 = (lane & 8) ? wx : w; w1 = (lane & 8) ? w : wx;
      asm volatile("" ::"v"(w0), "v"(w1));
    }
    STAMP(9);
    {
      u32x4 v[4];
      u32 spin = 0;
#pragma unroll 1
      for (; spin < SPIN_CAP; ++spin) {
        asm volatile("" ::: "memory");
#pragma unroll
        for (int i = 0; i < 4; ++i) v[i] = ldsc(rs, ioff + (i & 1) * 128 + (i >> 1) * 2048, 0);
        if (none(((badtag(v[0], tag) | badtag(v[1], tag)) | (badtag(v[2], tag) | badtag(v[3], tag))) != 0)) break;
      }
      STAMP(10); STAMPV(20, spin + 1);
      float ov[4];
#pragma unroll
      for (int i = 0; i < 4; ++i) ov[i] = asf(v[i >> 1][2 * (i & 1)]) * w0 + asf(v[2 + (i >> 1)][2 * (i & 1)]) * w1;
      SB();
#pragma unroll
      for (int i = 0; i < 4; ++i) ov[i] += dpp<XOR1>(ov[i]);
      SB();
#pragma unroll
      for (int i = 0; i < 4; ++i) ov[i] += dpp<XOR2>(ov[i]);
      SB();
#pragma unroll
      for (int i = 0; i < 4; ++i) ov[i] += dpp<HMIR>(ov[i]);
      SB();
      const u32x4 e = {cvtpk(ov[0], ov[0]), cvtpk(ov[1], ov[1]), cvtpk(ov[2], ov[2]), cvtpk(ov[3], ov[3])};
      *(LAS u32x4*)(L + L_OF + (lane >> 3) * 16) = e;
    }
    BARRIER();
    STAMP(11);
    if (b < 144) {
      u32x4 v[8];
      u32 spin = 0;
#pragma unroll 1
      for (; spin < SPIN_CAP; ++spin) {
        asm volatile("" ::: "memory");
        u32 bad = 0;
#pragma unroll
        for (int i = 0; i < 8; ++i) v[i] = ldsc(rs, yoff, i * 16 * 4608);
#pragma unroll
        for (int i = 0; i < 8; ++i) bad |= badtag(v[i], tag);
        if (none(bad != 0)) break;
      }
      STAMP(12); STAMPV(21, spin + 1);
      const float y0 = ((asf(v[0][0]) + asf(v[1][0])) + (asf(v[2][0]) + asf(v[3][0]))) + ((asf(v[4][0]) + asf(v[5][0])) + (asf(v[6][0]) + asf(v[7][0])));
      const float y1 = ((asf(v[0][2]) + asf(v[1][2])) + (asf(v[2][2]) + asf(v[3][2]))) + ((asf(v[4][2]) + asf(v[5][2])) + (asf(v[6][2]) + asf(v[7][2])));
      auto t0 = __builtin_amdgcn_permlane32_swap(__float_as_uint(y0), __float_as_uint(y1), false, false);
      float z = asf(t0[0]) + asf(t0[1]);
      auto t1 = __builtin_amdgcn_permlane16_swap(__float_as_uint(z), __float_as_uint(z), false, false);
      z = asf(t1[0]) + asf(t1[1]);
      z += dpp<ROR8>(z); z += dpp<ROR4>(z);
      if ((lane & 28) == 0) *(GAS unsigned short*)(out + (br2 * 576 + c0 + 2 * (lane & 3) + (lane >> 5)) * 2) = (unsigned short)cvtpk(z, z);
      if ((b | lane) == 0) *(GAS u32*)(ws + WS_EPOCH) = tag;
    }
    STAMP(13);
    FINISH();
  }
  const int a = wave - 1, br = a < 8;
  u32x4 K[16], V[20];
  const int row0 = (s & 1) * 288 + a * 32;
  const rsrc_t rq = mkrs(rbr ? wqB : wqA, a < 9 ? 576 * 8192 : 0);
  const u32 qoff = (u32)(row0 + 4 * (lane >> 3)) * 8192 + h * 512 + ((s >> 1) & 3) * 128 + (lane & 7) * 16;
#pragma unroll
  for (int i = 0; i < 4; ++i) V[i] = ldnt(rq, qoff, i * 8192);
  const u32x2 xv = __builtin_amdgcn_raw_buffer_load_b64(mkrs(rbr ? xB : xA, 1152), (row0 + 4 * (lane >> 3)) * 2, 0, 0);
  SB();
  const u32 kvo = (u32)(h * 8192 + (br ? s * 512 + a * 4 : 6144 + s * 128 + (a - 8) * 4)) * 256;
  const u32 kvb = br ? 131072 : 32768, sh = br ? 13 : 11;
  const rsrc_t rk = mkrs((br ? kB : kA) + kvo, kvb);
  const rsrc_t rv = mkrs((br ? vB : vA) + kvo, kvb);
  const u32 koff = (u32)((lane >> 2) & 3) * 256 + (lane & 3) * 64 + (lane >> 4) * 16, voff = lane * 16;
#define CHUNK(j) ((u32)((j) ^ s) << sh)
#pragma unroll
  for (int i = 0; i < KPRE; ++i) K[i] = ldnt(rk, koff, CHUNK(i));
  u32x4 msk;
#pragma unroll
  for (int k = 0; k < 4; ++k) msk[k] = ((lane & 7) >> 1) == k ? ((lane & 1) ? 0xffff0000u : 0x0000ffffu) : 0u;
  const int colp = (lane & 7) * 8 + ((lane >> 4) & 1) * 4 + (lane >> 5) * 2;
  SB();
  VMWAIT(KPRE);
#pragma unroll
  for (int i = KPRE; i < WIN; ++i) K[i] = ldnt(rk, koff, CHUNK(i));
  SB();
  {
    const u32x4 e = {__builtin_amdgcn_perm(xv[0], xv[0], 0x01000100u), __builtin_amdgcn_perm(xv[0], xv[0], 0x03020302u),
                     __builtin_amdgcn_perm(xv[1], xv[1], 0x01000100u), __builtin_amdgcn_perm(xv[1], xv[1], 0x03020302u)};
    const f32x2 qp = gemv_tile(e, msk, V);
    if (!(lane & 8) && a < 9) *(LAS f32x2*)(L + L_QP + a * 256 + colp * 4) = qp;
  }
  SB();
  STAMP(1);
#pragma unroll
  for (int i = (KPRE > WIN ? KPRE : WIN); i < 16; ++i) {
    VMWAIT(WIN - 1);
    K[i] = ldnt(rk, koff, CHUNK(i));
    SB();
  }
  STAMP(2);
#pragma unroll 1
  for (u32 spin = 0; spin < SPIN_CAP * 8; ++spin) { if (*(volatile LAS u32*)(L + L_FLAG) == 0) break; __builtin_amdgcn_s_sleep(1); }
  asm volatile("" ::: "memory");
  STAMP(3);
  const rsrc_t ro = mkrs((rbr ? woB : woA) + (u32)(h * 256 + (s & 7) * 32) * 1152, a < 9 ? 32 * 1152 : 0);
  const u32 wooff = (u32)(4 * (lane >> 3)) * 1152 + (lane & 7) * 16 + a * 128;
#define ISSUE(n) do { if ((n) < 16) V[n] = ldnt(rv, voff, CHUNK(n)); else if ((n) < 20) V[n] = ldnt(ro, wooff + ((n) - 16) * 1152, 0); } while (0)
  const u32x4 Q = *(LAS u32x4*)(L + L_Q + br * 512 + ((lane >> 3) & 1) * 256 + (lane & 3) * 64 + (lane >> 4) * 16);
  const u32 tag = *(LAS u32*)(L + L_FLAG + 12);
  float sc[16];
  u32 P[16];
  float m, l = 0.f, F;
  const float LOG2E = 1.4426950408889634f;
  const int yo = (lane & 8) ? WS_END : WS_Y + (rbr * 128 + h * 8 + (s & 7)) * 4608 + (a * 64 + colp) * 8;
  const int ooff = WS_O + (h * 16 + br * 8 + (lane >> 5) * 4 + ((lane & 15) >> 2)) * 4096 + s * 256 + (lane & 3) * 32 + ((lane >> 4) & 1) * 16;
  LAS unsigned char* cntp = L + L_FLAG + 8 - br * 4;
  f32x4 o = {0.f, 0.f, 0.f, 0.f};
#pragma unroll
  for (int n = 0; n < 16 + WIN; ++n) {
    u32x4 sel;
    if (n > 6) { sel = msk & P[n - WIN]; asm volatile("" : "+v"(sel)); }
    SB();
    if (n <= 20) VMWAIT(WIN - 1);
    if (n < 15 + WIN) ISSUE(n);
    SB();
    if (n < 4) {
      const int T = n;
      float d[4];
#pragma unroll
      for (int u = 0; u < 4; ++u) {
        const f32x4 t = mfma(K[T * 4 + u], Q, f32x4{0.f, 0.f, 0.f, 0.f});
        d[u] = ((dpp<0x00>(t[0]) + dpp<0x55>(t[1])) + dpp<0xAA>(t[2])) + dpp<0xFF>(t[3]);
      }
      const u32 r0 = cvtpk(d[0], d[1]), r1 = cvtpk(d[2], d[3]);
      const float SC = 0.08838834764831845f;
      const u32 r2 = cvtpk(bflo(r0) * SC, bfhi(r0) * SC), r3 = cvtpk(bflo(r1) * SC, bfhi(r1) * SC);
      sc[T * 4 + 0] = bflo(r2); sc[T * 4 + 1] = bfhi(r2); sc[T * 4 + 2] = bflo(r3); sc[T * 4 + 3] = bfhi(r3);
    } else if (n == 4) {
      m = sc[0];
#pragma unroll
      for (int i = 1; i < 16; ++i) m = fmaxf(m, sc[i]);
      m = x16_32_max(m);
#pragma unroll
      for (int i = 0; i < 6; ++i) { const float p = __builtin_amdgcn_exp2f((sc[i] - m) * LOG2E); l += p; P[i] = cvtpk(p, p); }
    } else if (n == 5) {
#pragma unroll
      for (int i = 6; i < 16; ++i) { const float p = __builtin_amdgcn_exp2f((sc[i] - m) * LOG2E); l += p; P[i] = cvtpk(p, p); }
      l = x16_32_sum(l);
      if ((lane & 55) == 0) *(LAS f32x2*)(L + L_ML + a * 16 + (lane >> 3) * 8) = f32x2{m * LOG2E, l};
      asm volatile("" ::"v"(yo), "v"(ooff), "v"(cntp));
      STAMP(4);
    } else if (n == 6) {
#pragma unroll
      for (int i = 0; i <= n - WIN; ++i) o = mfma(msk & P[i], V[i], o);
    } else {
      o = mfma(sel, V[n - WIN], o);
      if (n == 14) {
#pragma unroll 1
        for (u32 spin = 0; spin < SPIN_CAP * 8; ++spin) {
          const u32 f = *(volatile LAS u32*)(L + L_F + a * 8 + (lane >> 5) * 4);
          F = __uint_as_float(f);
          if (none(f == SENT)) break;
          __builtin_amdgcn_s_sleep(1);
        }
      }
    }
    SB();
  }
#pragma unroll
  for (int k = 15 + WIN; k < 20; ++k) ISSUE(k);
  SB();
  *(LAS f32x4*)(L + L_O + a * 1024 + lane * 16) = o * F;
  u32 arr;
  asm volatile("s_mov_b64 exec, 1\n\tds_add_rtn_u32 %0, %1, %2\n\ts_mov_b64 exec, -1\n\ts_waitcnt lgkmcnt(0)"
               : "=&v"(arr) : "v"(cntp), "v"(1u) : "memory");
  arr = __builtin_amdgcn_readfirstlane(arr);
  STAMP(5);
  if (arr == (br ? 6u : 0u)) {
    f32x4 t;
    if (br) {
      f32x4 u[8];
#pragma unroll
      for (int k = 0; k < 8; ++k) u[k] = *(LAS f32x4*)(L + L_O + k * 1024 + lane * 16);
      t = ((u[0] + u[1]) + (u[2] + u[3])) + ((u[4] + u[5]) + (u[6] + u[7]));
    } else {
      t = *(LAS f32x4*)(L + L_O + 8 * 1024 + lane * 16) + *(LAS f32x4*)(L + L_O + 9 * 1024 + lane * 16);
    }
    st2g(rs, ooff, t[0], t[1], tag);
    st2g(rs, ooff + 128, t[2], t[3], tag);
    STAMP(8);
  }
  if (a == 9) FINISH();
  BARRIER();
  const u32x4 e = *(LAS u32x4*)(L + L_OF + (lane >> 3) * 16);
  const f32x2 y = gemv_tile(e, msk, V + 16);
  st2g(rs, yo, y[0], y[1], tag);
  STAMP(7);
  FINISH();
}

extern "C" void kernel_launch(void* const* d_in, const int* in_sizes, int n_in, void* d_out, int out_size, void* d_ws,
                              size_t ws_size, hipStream_t stream) {
  typedef const unsigned char* P;
  if (OV & 1) tiny1<<<TINYG, TINYB, 0, stream>>>((unsigned char*)d_ws);
  fused_decode<<<256, NTHR, 0, stream>>>((P)d_in[0], (P)d_in[1], (P)d_in[2], (P)d_in[3], (P)d_in[4], (P)d_in[5], (P)d_in[6],
                                         (P)d_in[7], (P)d_in[8], (P)d_in[9], (unsigned char*)d_out, (unsigned char*)d_ws);
  if (OV & 2) tiny2<<<TINYG, TINYB, 0, stream>>>((unsigned char*)d_ws);
}
```

```cpp
#define OAUX 16
#include <hip/hip_runtime.h>
#include <hip/hip_bf16.h>
#include <stdint.h>

#define STAMPS 0
#define WIN 3
#define CODEPF 1
#define KPRE 0
#define QSLEEP 16

typedef unsigned int u32;
typedef unsigned long long u64;
typedef u32 u32x2 __attribute__((ext_vector_type(2)));
typedef u32 u32x4 __attribute__((ext_vector_type(4)));
typedef float f32x2 __attribute__((ext_vector_type(2)));
typedef float f32x4 __attribute__((ext_vector_type(4)));
typedef __bf16 bf16x2 __attribute__((ext_vector_type(2)));
typedef __bf16 bf16x8 __attribute__((ext_vector_type(8)));
#define GAS __attribute__((address_space(1)))
#define LAS __attribute__((address_space(3)))
typedef __amdgpu_buffer_rsrc_t rsrc_t;

constexpr int NWAVE = 11, NTHR = NWAVE * 64;
constexpr u32 SENT = 0xFFFFFFFFu;
constexpr u32 SPIN_CAP = 1u << 17;
constexpr int WS_EPOCH = 0;
constexpr int WS_Q = 4096;
constexpr int WS_ML = WS_Q + 256 * 512;
constexpr int WS_O = WS_ML + 16 * 1024;
constexpr int WS_Y = WS_O + 256 * 4096;
constexpr int WS_END = WS_Y + 256 * 4608;
constexpr int WS_STAMP = 4 << 20;
constexpr int L_QP = 0;
constexpr int L_ML = 2304;
constexpr int L_F = L_ML + 160;
constexpr int L_FLAG = L_F + 80;
constexpr int L_OF = 2816;
constexpr int L_Q = 3072;
constexpr int L_O = 4096;
constexpr int L_BYTES = L_O + 10 * 1024;
constexpr int SC1 = 16, NT = 2;
constexpr u32 CODE_BYTES = 7552;

__device__ __forceinline__ rsrc_t mkrs(const void* p, u32 bytes) { return __builtin_amdgcn_make_buffer_rsrc((void*)p, 0, bytes, 0x00020000); }
__device__ __forceinline__ u32x4 ldnt(rsrc_t r, u32 voff, u32 soff) { return __builtin_amdgcn_raw_buffer_load_b128(r, voff, soff, NT); }
__device__ __forceinline__ u32x4 ldsc(rsrc_t r, u32 voff, u32 soff) { return __builtin_amdgcn_raw_buffer_load_b128(r, voff, soff, SC1); }
__device__ __forceinline__ void st2g(rsrc_t r, u32 voff, float a, float b, u32 tag) {
  __builtin_amdgcn_raw_buffer_store_b128(u32x4{__float_as_uint(a), tag, __float_as_uint(b), tag}, r, voff, 0, SC1);
}
__device__ __forceinline__ u32 badtag(u32x4 v, u32 tag) { return (v[1] ^ tag) | (v[3] ^ tag); }
__device__ __forceinline__ f32x4 mfma(u32x4 a, u32x4 b, f32x4 c) {
  return __builtin_amdgcn_mfma_f32_16x16x32_bf16(__builtin_bit_cast(bf16x8, a), __builtin_bit_cast(bf16x8, b), c, 0, 0, 0);
}
__device__ __forceinline__ u32 cvtpk(float lo, float hi) {
  bf16x2 r = {(__bf16)lo, (__bf16)hi};
  return __builtin_bit_cast(u32, r);
}
__device__ __forceinline__ float bflo(u32 x) { return __uint_as_float(x << 16); }
__device__ __forceinline__ float bfhi(u32 x) { return __uint_as_float(x & 0xffff0000u); }
__device__ __forceinline__ float asf(u32 x) { return __uint_as_float(x); }
template <int CTRL> __device__ __forceinline__ float dpp(float x) {
  return __builtin_bit_cast(float, __builtin_amdgcn_mov_dpp(__builtin_bit_cast(int, x), CTRL, 0xf, 0xf, true));
}
constexpr int XOR1 = 0xB1, XOR2 = 0x4E, HMIR = 0x141, ROR8 = 0x128, ROR4 = 0x124;
__device__ __forceinline__ float x16_32_max(float x) {
  auto s = __builtin_amdgcn_permlane16_swap(__float_as_uint(x), __float_as_uint(x), false, false);
  x = fmaxf(__uint_as_float(s[0]), __uint_as_float(s[1]));
  auto t = __builtin_amdgcn_permlane32_swap(__float_as_uint(x), __float_as_uint(x), false, false);
  return fmaxf(__uint_as_float(t[0]), __uint_as_float(t[1]));
}
__device__ __forceinline__ float x16_32_sum(float x) {
  auto s = __builtin_amdgcn_permlane16_swap(__float_as_uint(x), __float_as_uint(x), false, false);
  x = __uint_as_float(s[0]) + __uint_as_float(s[1]);
  auto t = __builtin_amdgcn_permlane32_swap(__float_as_uint(x), __float_as_uint(x), false, false);
  return __uint_as_float(t[0]) + __uint_as_float(t[1]);
}
__device__ __forceinline__ bool none(bool p) { return __builtin_amdgcn_ballot_w64(p) == 0; }
__device__ __forceinline__ f32x2 gemv_tile(u32x4 e, u32x4 msk, const u32x4* w) {
  f32x4 y = {0.f, 0.f, 0.f, 0.f};
#pragma unroll
  for (int i = 0; i < 4; ++i) y = mfma(msk & e[i], w[i], y);
  auto t0 = __builtin_amdgcn_permlane32_swap(__float_as_uint(y[0]), __float_as_uint(y[2]), false, false);
  auto t1 = __builtin_amdgcn_permlane32_swap(__float_as_uint(y[1]), __float_as_uint(y[3]), false, false);
  return f32x2{__uint_as_float(t0[0]) + dpp<ROR8>(__uint_as_float(t0[1])), __uint_as_float(t1[0]) + dpp<ROR8>(__uint_as_float(t1[1]))};
}
#define SB() __builtin_amdgcn_sched_barrier(0)
#define VMWAIT(n) asm volatile("s_waitcnt vmcnt(%0)" ::"n"(n) : "memory")
#define LGKWAIT() asm volatile("s_waitcnt lgkmcnt(0)" ::: "memory")
#define BARRIER() asm volatile("s_waitcnt lgkmcnt(0)\n\ts_barrier" ::: "memory")
#define STAMP(i) do {} while (0)
#define STAMPV(i, v) do {} while (0)
#define FINISH() __builtin_amdgcn_endpgm()

__global__ void __launch_bounds__(NTHR, 3)
fused_decode(const unsigned char* xA, const unsigned char* kA, const unsigned char* vA, const unsigned char* wqA,
             const unsigned char* woA, const unsigned char* xB, const unsigned char* kB, const unsigned char* vB,
             const unsigned char* wqB, const unsigned char* woB, unsigned char* out_, unsigned char* ws_) {
  __shared__ __attribute__((aligned(16))) unsigned char smem[L_BYTES];
  LAS unsigned char* L = (LAS unsigned char*)smem;
  u64 pc;
  asm volatile("s_getpc_b64 %0" : "=s"(pc));
  GAS unsigned char* out = (GAS unsigned char*)out_;
  GAS unsigned char* ws = (GAS unsigned char*)ws_;
  const int b = blockIdx.x, lane = threadIdx.x & 63;
  const int wave = __builtin_amdgcn_readfirstlane(threadIdx.x >> 6);
  const int h = (b & 7) * 2 + (b >> 7), s = (b >> 3) & 15, wg = h * 16 + s;
  const rsrc_t rs = mkrs(ws_, WS_END);
  STAMP(0);
  *(LAS u32*)(L + threadIdx.x * 4) = SENT;
  BARRIER();
  asm volatile("" ::"s"(xA), "s"(kA), "s"(vA), "s"(wqA), "s"(woA), "s"(xB), "s"(kB), "s"(vB), "s"(wqB), "s"(woB), "s"(out_), "s"(ws_));
  const int rbr = s >> 3;
  if (wave == 0) {
    const u32 tag = __builtin_amdgcn_raw_buffer_load_b32(rs, WS_EPOCH, 0, SC1) + 1u;
    {
      const rsrc_t rc = mkrs((const void*)(pc & ~(u64)255), CODE_BYTES);
      const u32 c0 = __builtin_amdgcn_raw_buffer_load_b32(rc, lane * 128, 0, 0);
      asm volatile("" ::"v"(c0));
    }
    *(LAS u32*)(L + L_FLAG + 12) = tag;
    STAMP(1);
    {
      u32 t[9];
#pragma unroll 1
      for (u32 spin = 0; spin < SPIN_CAP * 8; ++spin) {
#pragma unroll
        for (int a = 0; a < 9; ++a) t[a] = *(volatile LAS u32*)(L + L_QP + a * 256 + lane * 4);
        if (none(max(max(max(max(t[0], t[1]), max(t[2], t[3])), max(max(t[4], t[5]), max(t[6], t[7]))), t[8]) == SENT)) break;
        __builtin_amdgcn_s_sleep(1);
      }
      const float qp = ((asf(t[0]) + asf(t[1])) + (asf(t[2]) + asf(t[3]))) + ((asf(t[4]) + asf(t[5])) + (asf(t[6]) + asf(t[7]))) + asf(t[8]);
      __builtin_amdgcn_raw_buffer_store_b64(u32x2{__float_as_uint(qp), tag}, rs, WS_Q + wg * 512 + lane * 8, 0, SC1);
    }
    STAMP(2);
    VMWAIT(0);
    STAMP(3);
    {
      u32x4 v[8];
      u32 spin = 0;
#pragma unroll 1
      for (; spin < SPIN_CAP; ++spin) {
        __builtin_amdgcn_s_sleep(QSLEEP);
        asm volatile("" ::: "memory");
        u32 bad = 0;
#pragma unroll
        for (int i = 0; i < 8; ++i) v[i] = ldsc(rs, WS_Q + h * 8192 + (lane >> 5) * 1024 + (lane & 31) * 16 + (i & 1) * 512, (i >> 1) * 2048);
#pragma unroll
        for (int i = 0; i < 8; ++i) bad |= badtag(v[i], tag);
        if (none(bad != 0)) break;
      }
      STAMPV(22, spin + 1);
#pragma unroll
      for (int i = 0; i < 4; ++i)
        *(LAS u32*)(L + L_Q + i * 256 + lane * 4) = cvtpk(asf(v[2 * i][0]) + asf(v[2 * i + 1][0]), asf(v[2 * i][2]) + asf(v[2 * i + 1][2]));
      LGKWAIT();
      *(volatile LAS u32*)(L + L_FLAG) = 0;
    }
    STAMP(4);
    {
      u32x2 ml;
#pragma unroll 1
      for (u32 spin = 0; spin < SPIN_CAP * 8; ++spin) {
        ml = *(volatile LAS u32x2*)(L + L_ML + (lane < 20 ? lane : 0) * 8);
        if (none(max(ml[0], ml[1]) == SENT)) break;
        __builtin_amdgcn_s_sleep(2);
      }
      STAMP(6);
      const bool live = lane < 20;
      float m = live ? asf(ml[0]) : -INFINITY;
      float M = fmaxf(m, dpp<XOR2>(m)); M = fmaxf(M, dpp<ROR4>(M)); M = fmaxf(M, dpp<ROR8>(M));
      const float F = __builtin_amdgcn_exp2f(m - M);
      if (live) *(LAS float*)(L + L_F + lane * 4) = F;
      float l = live ? asf(ml[1]) * F : 0.f;
      l += dpp<XOR2>(l); l += dpp<ROR4>(l); l += dpp<ROR8>(l);
      st2g(rs, (lane & 46) == 0 ? WS_ML + h * 1024 + ((lane < 16 ? 2 : 0) + (lane & 1)) * 256 + s * 16 : WS_END, M, l, tag);
    }
    const int moff = WS_ML + h * 1024 + (s >> 2) * 256 + (lane & 15) * 16;
    const int ioff = WS_O + wg * 4096 + (lane & 7) * 256 + (lane >> 3) * 16;
    const int br2 = b >= 72, c0 = (b - br2 * 72) * 8;
    const int yoff = WS_Y + (br2 * 128 + (lane >> 2)) * 4608 + c0 * 8 + (lane & 3) * 16;
    asm volatile("" ::"v"(yoff), "v"(ioff), "v"(moff));
    STAMP(7);
#pragma unroll 1
    for (u32 spin = 0; spin < SPIN_CAP * 8; ++spin) {
      if (*(volatile LAS u32*)(L + L_FLAG + 4) != SENT) break;
      __builtin_amdgcn_s_sleep(1);
    }
    STAMP(8);
    float w0, w1;
    {
      u32x4 gv;
#pragma unroll 1
      for (u32 spin = 0; spin < SPIN_CAP; ++spin) {
        asm volatile("" ::: "memory");
        gv = ldsc(rs, moff, 0);
        if (none(badtag(gv, tag) != 0)) break;
      }
      const float gm = asf(gv[0]);
      float GM = fmaxf(gm, dpp<XOR1>(gm)); GM = fmaxf(GM, dpp<XOR2>(GM)); GM = fmaxf(GM, dpp<HMIR>(GM)); GM = fmaxf(GM, dpp<ROR8>(GM));
      const float e = __builtin_amdgcn_exp2f(gm - GM);
      float GL = asf(gv[2]) * e;
      GL += dpp<XOR1>(GL); GL += dpp<XOR2>(GL); GL += dpp<HMIR>(GL); GL += dpp<ROR8>(GL);
      const float w = e / GL, wx = dpp<ROR8>(w);
      w0 = (lane & 8) ? wx : w; w1 = (lane & 8) ? w : wx;
      asm volatile("" ::"v"(w0), "v"(w1));
    }
    STAMP(9);
    {
      u32x4 v[4];
      u32 spin = 0;
#pragma unroll 1
      for (; spin < SPIN_CAP; ++spin) {
        asm volatile("" ::: "memory");
#pragma unroll
        for (int i = 0; i < 4; ++i) v[i] = ldsc(rs, ioff + (i & 1) * 128 + (i >> 1) * 2048, 0);
        if (none(((badtag(v[0], tag) | badtag(v[1], tag)) | (badtag(v[2], tag) | badtag(v[3], tag))) != 0)) break;
      }
      STAMP(10); STAMPV(20, spin + 1);
      float ov[4];
#pragma unroll
      for (int i = 0; i < 4; ++i) ov[i] = asf(v[i >> 1][2 * (i & 1)]) * w0 + asf(v[2 + (i >> 1)][2 * (i & 1)]) * w1;
      SB();
#pragma unroll
      for (int i = 0; i < 4; ++i) ov[i] += dpp<XOR1>(ov[i]);
      SB();
#pragma unroll
      for (int i = 0; i < 4; ++i) ov[i] += dpp<XOR2>(ov[i]);
      SB();
#pragma unroll
      for (int i = 0; i < 4; ++i) ov[i] += dpp<HMIR>(ov[i]);
      SB();
      const u32x4 e = {cvtpk(ov[0], ov[0]), cvtpk(ov[1], ov[1]), cvtpk(ov[2], ov[2]), cvtpk(ov[3], ov[3])};
      *(LAS u32x4*)(L + L_OF + (lane >> 3) * 16) = e;
    }
    BARRIER();
    STAMP(11);
    if (b < 144) {
      u32x4 v[8];
      u32 spin = 0;
#pragma unroll 1
      for (; spin < SPIN_CAP; ++spin) {
        asm volatile("" ::: "memory");
        u32 bad = 0;
#pragma unroll
        for (int i = 0; i < 8; ++i) v[i] = ldsc(rs, yoff, i * 16 * 4608);
#pragma unroll
        for (int i = 0; i < 8; ++i) bad |= badtag(v[i], tag);
        if (none(bad != 0)) break;
      }
      STAMP(12); STAMPV(21, spin + 1);
      const float y0 = ((asf(v[0][0]) + asf(v[1][0])) + (asf(v[2][0]) + asf(v[3][0]))) + ((asf(v[4][0]) + asf(v[5][0])) + (asf(v[6][0]) + asf(v[7][0])));
      const float y1 = ((asf(v[0][2]) + asf(v[1][2])) + (asf(v[2][2]) + asf(v[3][2]))) + ((asf(v[4][2]) + asf(v[5][2])) + (asf(v[6][2]) + asf(v[7][2])));
      auto t0 = __builtin_amdgcn_permlane32_swap(__float_as_uint(y0), __float_as_uint(y1), false, false);
      float z = asf(t0[0]) + asf(t0[1]);
      auto t1 = __builtin_amdgcn_permlane16_swap(__float_as_uint(z), __float_as_uint(z), false, false);
      z = asf(t1[0]) + asf(t1[1]);
      z += dpp<ROR8>(z); z += dpp<ROR4>(z);
      __builtin_amdgcn_raw_buffer_store_b16((unsigned short)cvtpk(z, z), mkrs(out_, 2304), (lane & 28) == 0 ? (br2 * 576 + c0 + 2 * (lane & 3) + (lane >> 5)) * 2 : 4096, 0, OAUX);
      __builtin_amdgcn_raw_buffer_store_b32(tag, rs, (b | lane) == 0 ? WS_EPOCH : WS_END, 0, OAUX);
    }
    STAMP(13);
    FINISH();
  }
  const int a = wave - 1, br = a < 8;
  u32x4 K[16], V[20];
  const int row0 = (s & 1) * 288 + a * 32;
  const rsrc_t rq = mkrs(rbr ? wqB : wqA, a < 9 ? 576 * 8192 : 0);
  const u32 qoff = (u32)(row0 + 4 * (lane >> 3)) * 8192 + h * 512 + ((s >> 1) & 3) * 128 + (lane & 7) * 16;
#pragma unroll
  for (int i = 0; i < 4; ++i) V[i] = ldnt(rq, qoff, i * 8192);
  const u32x2 xv = __builtin_amdgcn_raw_buffer_load_b64(mkrs(rbr ? xB : xA, 1152), (row0 + 4 * (lane >> 3)) * 2, 0, 0);
  SB();
  const u32 kvo = (u32)(h * 8192 + (br ? s * 512 + a * 4 : 6144 + s * 128 + (a - 8) * 4)) * 256;
  const u32 kvb = br ? 131072 : 32768, sh = br ? 13 : 11;
  const rsrc_t rk = mkrs((br ? kB : kA) + kvo, kvb);
  const rsrc_t rv = mkrs((br ? vB : vA) + kvo, kvb);
  const u32 koff = (u32)((lane >> 2) & 3) * 256 + (lane & 3) * 64 + (lane >> 4) * 16, voff = lane * 16;
#define CHUNK(j) ((u32)((j) ^ s) << sh)
#pragma unroll
  for (int i = 0; i < KPRE; ++i) K[i] = ldnt(rk, koff, CHUNK(i));
  u32x4 msk;
#pragma unroll
  for (int k = 0; k < 4; ++k) msk[k] = ((lane & 7) >> 1) == k ? ((lane & 1) ? 0xffff0000u : 0x0000ffffu) : 0u;
  const int colp = (lane & 7) * 8 + ((lane >> 4) & 1) * 4 + (lane >> 5) * 2;
  SB();
  VMWAIT(KPRE);
#pragma unroll
  for (int i = KPRE; i < WIN; ++i) K[i] = ldnt(rk, koff, CHUNK(i));
  SB();
  {
    const u32x4 e = {__builtin_amdgcn_perm(xv[0], xv[0], 0x01000100u), __builtin_amdgcn_perm(xv[0], xv[0], 0x03020302u),
                     __builtin_amdgcn_perm(xv[1], xv[1], 0x01000100u), __builtin_amdgcn_perm(xv[1], xv[1], 0x03020302u)};
    const f32x2 qp = gemv_tile(e, msk, V);
    if (!(lane & 8) && a < 9) *(LAS f32x2*)(L + L_QP + a * 256 + colp * 4) = qp;
  }
  SB();
  STAMP(1);
#pragma unroll
  for (int i = (KPRE > WIN ? KPRE : WIN); i < 16; ++i) {
    VMWAIT(WIN - 1);
    K[i] = ldnt(rk, koff, CHUNK(i));
    SB();
  }
  STAMP(2);
#pragma unroll 1
  for (u32 spin = 0; spin < SPIN_CAP * 8; ++spin) { if (*(volatile LAS u32*)(L + L_FLAG) == 0) break; __builtin_amdgcn_s_sleep(1); }
  asm volatile("" ::: "memory");
  STAMP(3);
  const rsrc_t ro = mkrs((rbr ? woB : woA) + (u32)(h * 256 + (s & 7) * 32) * 1152, a < 9 ? 32 * 1152 : 0);
  const u32 wooff = (u32)(4 * (lane >> 3)) * 1152 + (lane & 7) * 16 + a * 128;
#define ISSUE(n) do { if ((n) < 16) V[n] = ldnt(rv, voff, CHUNK(n)); else if ((n) < 20) V[n] = ldnt(ro, wooff + ((n) - 16) * 1152, 0); } while (0)
  const u32x4 Q = *(LAS u32x4*)(L + L_Q + br * 512 + ((lane >> 3) & 1) * 256 + (lane & 3) * 64 + (lane >> 4) * 16);
  const u32 tag = *(LAS u32*)(L + L_FLAG + 12);
  float sc[16];
  u32 P[16];
  float m, l = 0.f, F;
  const float LOG2E = 1.4426950408889634f;
  const int yo = (lane & 8) ? WS_END : WS_Y + (rbr * 128 + h * 8 + (s & 7)) * 4608 + (a * 64 + colp) * 8;
  const int ooff = WS_O + (h * 16 + br * 8 + (lane >> 5) * 4 + ((lane & 15) >> 2)) * 4096 + s * 256 + (lane & 3) * 32 + ((lane >> 4) & 1) * 16;
  LAS unsigned char* cntp = L + L_FLAG + 8 - br * 4;
  f32x4 o = {0.f, 0.f, 0.f, 0.f};
#pragma unroll
  for (int n = 0; n < 16 + WIN; ++n) {
    u32x4 sel;
    if (n > 6) { sel = msk & P[n - WIN]; asm volatile("" : "+v"(sel)); }
    SB();
    if (n <= 20) VMWAIT(WIN - 1);
    if (n < 15 + WIN) ISSUE(n);
    SB();
    if (n < 4) {
      const int T = n;
      float d[4];
#pragma unroll
      for (int u = 0; u < 4; ++u) {
        const f32x4 t = mfma(K[T * 4 + u], Q, f32x4{0.f, 0.f, 0.f, 0.f});
        d[u] = ((dpp<0x00>(t[0]) + dpp<0x55>(t[1])) + dpp<0xAA>(t[2])) + dpp<0xFF>(t[3]);
      }
      const u32 r0 = cvtpk(d[0], d[1]), r1 = cvtpk(d[2], d[3]);
      const float SC = 0.08838834764831845f;
      const u32 r2 = cvtpk(bflo(r0) * SC, bfhi(r0) * SC), r3 = cvtpk(bflo(r1) * SC, bfhi(r1) * SC);
      sc[T * 4 + 0] = bflo(r2); sc[T * 4 + 1] = bfhi(r2); sc[T * 4 + 2] = bflo(r3); sc[T * 4 + 3] = bfhi(r3);
    } else if (n == 4) {
      m = sc[0];
#pragma unroll
      for (int i = 1; i < 16; ++i) m = fmaxf(m, sc[i]);
      m = x16_32_max(m);
#pragma unroll
      for (int i = 0; i < 6; ++i) { const float p = __builtin_amdgcn_exp2f((sc[i] - m) * LOG2E); l += p; P[i] = cvtpk(p, p); }
    } else if (n == 5) {
#pragma unroll
      for (int i = 6; i < 16; ++i) { const float p = __builtin_amdgcn_exp2f((sc[i] - m) * LOG2E); l += p; P[i] = cvtpk(p, p); }
      l = x16_32_sum(l);
      if ((lane & 55) == 0) *(LAS f32x2*)(L + L_ML + a * 16 + (lane >> 3) * 8) = f32x2{m * LOG2E, l};
      asm volatile("" ::"v"(yo), "v"(ooff), "v"(cntp));
      STAMP(4);
    } else if (n == 6) {
#pragma unroll
      for (int i = 0; i <= n - WIN; ++i) o = mfma(msk & P[i], V[i], o);
    } else {
      o = mfma(sel, V[n - WIN], o);
      if (n == 14) {
#pragma unroll 1
        for (u32 spin = 0; spin < SPIN_CAP * 8; ++spin) {
          const u32 f = *(volatile LAS u32*)(L + L_F + a * 8 + (lane >> 5) * 4);
          F = __uint_as_float(f);
          if (none(f == SENT)) break;
          __builtin_amdgcn_s_sleep(1);
        }
      }
    }
    SB();
  }
#pragma unroll
  for (int k = 15 + WIN; k < 20; ++k) ISSUE(k);
  SB();
  *(LAS f32x4*)(L + L_O + a * 1024 + lane * 16) = o * F;
  u32 arr;
  asm volatile("s_mov_b64 exec, 1\n\tds_add_rtn_u32 %0, %1, %2\n\ts_mov_b64 exec, -1\n\ts_waitcnt lgkmcnt(0)"
               : "=&v"(arr) : "v"(cntp), "v"(1u) : "memory");
  arr = __builtin_amdgcn_readfirstlane(arr);
  STAMP(5);
  if (arr == (br ? 6u : 0u)) {
    f32x4 t;
    if (br) {
      f32x4 u[8];
#pragma unroll
      for (int k = 0; k < 8; ++k) u[k] = *(LAS f32x4*)(L + L_O + k * 1024 + lane * 16);
      t = ((u[0] + u[1]) + (u[2] + u[3])) + ((u[4] + u[5]) + (u[6] + u[7]));
    } else {
      t = *(LAS f32x4*)(L + L_O + 8 * 1024 + lane * 16) + *(LAS f32x4*)(L + L_O + 9 * 1024 + lane * 16);
    }
    st2g(rs, ooff, t[0], t[1], tag);
    st2g(rs, ooff + 128, t[2], t[3], tag);
    STAMP(8);
  }
  if (a == 9) FINISH();
  BARRIER();
  const u32x4 e = *(LAS u32x4*)(L + L_OF + (lane >> 3) * 16);
  const f32x2 y = gemv_tile(e, msk, V + 16);
  st2g(rs, yo, y[0], y[1], tag);
  STAMP(7);
  FINISH();
}

extern "C" void kernel_launch(void* const* d_in, const int* in_sizes, int n_in, void* d_out, int out_size, void* d_ws,
                              size_t ws_size, hipStream_t stream) {
  typedef const unsigned char* P;
  fused_decode<<<256, NTHR, 0, stream>>>((P)d_in[0], (P)d_in[1], (P)d_in[2], (P)d_in[3], (P)d_in[4], (P)d_in[5], (P)d_in[6],
                                         (P)d_in[7], (P)d_in[8], (P)d_in[9], (unsigned char*)d_out, (unsigned char*)d_ws);
}
```

```cpp
#define DS 0
#define DW 0
#define DK 0
#define DM 0
#include <hip/hip_runtime.h>
#include <hip/hip_bf16.h>
#include <stdint.h>

#define STAMPS 0
#define WIN 3
#define CODEPF 1
#define QSLEEP 16
#define QOFF 0
#define STARTM 1
#define WOM 0
#define WEND WIN

typedef unsigned int u32;
typedef unsigned long long u64;
typedef u32 u32x2 __attribute__((ext_vector_type(2)));
typedef u32 u32x4 __attribute__((ext_vector_type(4)));
typedef float f32x2 __attribute__((ext_vector_type(2)));
typedef float f32x4 __attribute__((ext_vector_type(4)));
typedef __bf16 bf16x2 __attribute__((ext_vector_type(2)));
typedef __bf16 bf16x8 __attribute__((ext_vector_type(8)));
#define GAS __attribute__((address_space(1)))
#define LAS __attribute__((address_space(3)))
typedef __amdgpu_buffer_rsrc_t rsrc_t;

constexpr int NWAVE = 11, NTHR = NWAVE * 64;
constexpr u32 SENT = 0xFFFFFFFFu;
constexpr u32 SPIN_CAP = 1u << 17;
constexpr int WS_EPOCH = 0;
constexpr int WS_Q = 4096;
constexpr int WS_ML = WS_Q + 256 * 512;
constexpr int WS_O = WS_ML + 16 * 1024;
constexpr int WS_Y = WS_O + 256 * 2048;
constexpr int WS_END = WS_Y + 256 * 2304;
constexpr int WS_STAMP = 4 << 20;
constexpr int L_QP = 0;
constexpr int L_ML = 2304;
constexpr int L_F = L_ML + 160;
constexpr int L_FLAG = L_F + 80;
constexpr int L_OF = 2816;
constexpr int L_Q = 3072;
constexpr int L_O = 4096;
constexpr int L_BYTES = L_O + 10 * 1024;
constexpr int SC1 = 16, NT = 2;
constexpr u32 CODE_BYTES = 7936;

__device__ __forceinline__ rsrc_t mkrs(const void* p, u32 bytes) { return __builtin_amdgcn_make_buffer_rsrc((void*)p, 0, bytes, 0x00020000); }
__device__ __forceinline__ u32x4 ldnt(rsrc_t r, u32 voff, u32 soff) { return __builtin_amdgcn_raw_buffer_load_b128(r, voff, soff, NT); }
__device__ __forceinline__ u32x4 ldsc(rsrc_t r, u32 voff, u32 soff) { return __builtin_amdgcn_raw_buffer_load_b128(r, voff, soff, SC1); }
__device__ __forceinline__ u32 badtag(u32x4 v, u32 tag) { return (v[1] ^ tag) | (v[3] ^ tag); }
__device__ __forceinline__ u32 tw(float x, u32 tag) { return __builtin_amdgcn_perm(__float_as_uint(x), tag, 0x07060500u); }
__device__ __forceinline__ float twval(u32 w) { return __uint_as_float(w & 0xffffff00u); }
__device__ __forceinline__ u32 twtags(u32x4 v) {
  return __builtin_amdgcn_perm(__builtin_amdgcn_perm(v[3], v[2], 0x0c0c0400u), __builtin_amdgcn_perm(v[1], v[0], 0x0c0c0400u), 0x05040100u);
}
__device__ __forceinline__ f32x4 mfma(u32x4 a, u32x4 b, f32x4 c) {
  return __builtin_amdgcn_mfma_f32_16x16x32_bf16(__builtin_bit_cast(bf16x8, a), __builtin_bit_cast(bf16x8, b), c, 0, 0, 0);
}
__device__ __forceinline__ u32 cvtpk(float lo, float hi) {
  bf16x2 r = {(__bf16)lo, (__bf16)hi};
  return __builtin_bit_cast(u32, r);
}
__device__ __forceinline__ float bflo(u32 x) { return __uint_as_float(x << 16); }
__device__ __forceinline__ float bfhi(u32 x) { return __uint_as_float(x & 0xffff0000u); }
__device__ __forceinline__ float asf(u32 x) { return __uint_as_float(x); }
template <int CTRL> __device__ __forceinline__ float dpp(float x) {
  return __builtin_bit_cast(float, __builtin_amdgcn_mov_dpp(__builtin_bit_cast(int, x), CTRL, 0xf, 0xf, true));
}
constexpr int XOR1 = 0xB1, XOR2 = 0x4E, HMIR = 0x141, ROR8 = 0x128, ROR4 = 0x124;
__device__ __forceinline__ float x16_32_max(float x) {
  auto s = __builtin_amdgcn_permlane16_swap(__float_as_uint(x), __float_as_uint(x), false, false);
  x = fmaxf(__uint_as_float(s[0]), __uint_as_float(s[1]));
  auto t = __builtin_amdgcn_permlane32_swap(__float_as_uint(x), __float_as_uint(x), false, false);
  return fmaxf(__uint_as_float(t[0]), __uint_as_float(t[1]));
}
__device__ __forceinline__ float x16_32_sum(float x) {
  auto s = __builtin_amdgcn_permlane16_swap(__float_as_uint(x), __float_as_uint(x), false, false);
  x = __uint_as_float(s[0]) + __uint_as_float(s[1]);
  auto t = __builtin_amdgcn_permlane32_swap(__float_as_uint(x), __float_as_uint(x), false, false);
  return __uint_as_float(t[0]) + __uint_as_float(t[1]);
}
__device__ __forceinline__ bool none(bool p) { return __builtin_amdgcn_ballot_w64(p) == 0; }
__device__ __forceinline__ f32x2 gemv_tile(u32x4 e, u32x4 msk, const u32x4* w) {
  f32x4 y = {0.f, 0.f, 0.f, 0.f};
#pragma unroll
  for (int i = 0; i < 4; ++i) y = mfma(msk & e[i], w[i], y);
  auto t0 = __builtin_amdgcn_permlane32_swap(__float_as_uint(y[0]), __float_as_uint(y[2]), false, false);
  auto t1 = __builtin_amdgcn_permlane32_swap(__float_as_uint(y[1]), __float_as_uint(y[3]), false, false);
  return f32x2{__uint_as_float(t0[0]) + dpp<ROR8>(__uint_as_float(t0[1])), __uint_as_float(t1[0]) + dpp<ROR8>(__uint_as_float(t1[1]))};
}
template <int I> struct IC { static constexpr int v = I; };
template <int LO, int HI, class Fn> __device__ __forceinline__ void sfor(Fn&& f) {
  if constexpr (LO < HI) { f(IC<LO>{}); sfor<LO + 1, HI>(f); }
}
constexpr int issued_after(int n) {
  if (n < 0) return 0;
  const int nlast = WOM == 0 ? 20 : 16;
  if (n >= 15 + WIN) n = 14 + WIN;
  int u = n + 1;
  if (n + 1 + 2 * (WEND - WIN) >= 16 && n < 16) { u = n + 1 + WEND - WIN; if (u > 16) u = 16; }
  return u < nlast ? u : nlast;
}
#define SB() __builtin_amdgcn_sched_barrier(0)
#define VMWAIT(n) asm volatile("s_waitcnt vmcnt(%0)" ::"n"(n) : "memory")
#define LGKWAIT() asm volatile("s_waitcnt lgkmcnt(0)" ::: "memory")
#define BARRIER() asm volatile("s_waitcnt lgkmcnt(0)\n\ts_barrier" ::: "memory")
#define BAR() asm volatile("s_barrier" ::: "memory")
#define RT(x) asm volatile("s_memrealtime %0\n\ts_waitcnt lgkmcnt(0)" : "=s"(x)::"memory")
#define STAMP(i) do { if ((i) == DS && wave == DW) RT(tdiag); } while (0)
#define STAMPV(i, v) do {} while (0)
#define FINISH() do { GAS u64* sp_ = (GAS u64*)((GAS unsigned char*)ws_ + (6 << 20)); \
  if (wave == DW && lane == 0) { __hip_atomic_store(sp_ + b, tstart, __ATOMIC_RELAXED, __HIP_MEMORY_SCOPE_AGENT); __hip_atomic_store(sp_ + 256 + b, tdiag, __ATOMIC_RELAXED, __HIP_MEMORY_SCOPE_AGENT); } \
  if (wave == 0 && b == 0) { u64 t; do { __builtin_amdgcn_s_sleep(4); RT(t); } while (t < tstart + 3500); \
    u64 mn = ~0ull, mx = 0, dn = ~0ull, sum = 0; for (int i = 0; i < 4; ++i) { u64 v = __hip_atomic_load(sp_ + i * 64 + lane, __ATOMIC_RELAXED, __HIP_MEMORY_SCOPE_AGENT); u64 d = __hip_atomic_load(sp_ + 256 + i * 64 + lane, __ATOMIC_RELAXED, __HIP_MEMORY_SCOPE_AGENT); mn = v < mn ? v : mn; mx = d > mx ? d : mx; dn = d < dn ? d : dn; sum += d; } \
    for (int m_ = 1; m_ < 64; m_ <<= 1) { u64 o1 = __shfl_xor(mn, m_), o2 = __shfl_xor(mx, m_), o3 = __shfl_xor(dn, m_), o4 = __shfl_xor(sum, m_); mn = o1 < mn ? o1 : mn; mx = o2 > mx ? o2 : mx; dn = o3 < dn ? o3 : dn; sum += o4; } \
    u64 xv_ = DK == 0 ? mx - mn : DK == 1 ? dn - mn : sum / 256 - mn; if (xv_ > 4000) xv_ = 4000; \
    do { __builtin_amdgcn_s_sleep(4); RT(t); } while (t < mn + 5000 + DM * xv_); } __builtin_amdgcn_endpgm(); } while (0)

__global__ void __launch_bounds__(NTHR, 3)
fused_decode(const unsigned char* xA, const unsigned char* kA, const unsigned char* vA, const unsigned char* wqA,
             const unsigned char* woA, const unsigned char* xB, const unsigned char* kB, const unsigned char* vB,
             const unsigned char* wqB, const unsigned char* woB, unsigned char* out_, unsigned char* ws_) {
  __shared__ __attribute__((aligned(16))) unsigned char smem[L_BYTES];
  LAS unsigned char* L = (LAS unsigned char*)smem;
  u64 pc;
  asm volatile("s_getpc_b64 %0" : "=s"(pc));
  const int b = blockIdx.x, lane = threadIdx.x & 63;
  const int wave = __builtin_amdgcn_readfirstlane(threadIdx.x >> 6);
  const int x = b & 7, j = b >> 3;
#define HEAD_OF(x_, k_) ((((x_) & 1) ^ 1) * 8 + ((x_) >> 1) * 2 + (k_))
  const int h = HEAD_OF(x, j >> 4), s = j & 15;
  const int rbr = x >> 2, rh = (x >> 1) & 1, cb = (x & 1) * 32 + j;
  const int xs = x ^ (1 + 3 * (j >> 4));
  const int hc = HEAD_OF(xs, (j >> 3) & 1), rc = j & 7;
  const rsrc_t rs = mkrs(ws_, WS_END);
  u64 tstart, tdiag; RT(tstart); tdiag = tstart;
  *(LAS u32*)(L + threadIdx.x * 4) = SENT;
  BARRIER();
  asm volatile("" ::"s"(xA), "s"(kA), "s"(vA), "s"(wqA), "s"(woA), "s"(xB), "s"(kB), "s"(vB), "s"(wqB), "s"(woB), "s"(out_), "s"(ws_));
  const int a = wave - 1, br = a < 8;
  u32x4 K[16], V[20];
  const int row0 = rh * 288 + a * 32;
  const rsrc_t rq = mkrs(rbr ? wqB : wqA, (u32)a < 9u ? 576 * 8192 : 0);
  const u32 qoff = (u32)(row0 + 4 * (lane >> 3)) * 8192 + cb * 128 + (lane & 7) * 16;
  const u32x2 xv = __builtin_amdgcn_raw_buffer_load_b64(mkrs(rbr ? xB : xA, (u32)a < 9u ? 1152 : 0), (row0 + 4 * (lane >> 3)) * 2, 0, 0);
#pragma unroll
  for (int i = 0; i < WIN; ++i) { if (i) BAR(); V[i] = ldnt(rq, qoff, i * 8192); }
  SB();
  if (wave == 0) {
    const u32 tag = __builtin_amdgcn_raw_buffer_load_b32(rs, WS_EPOCH, 0, SC1) + 1u;
    {
      const rsrc_t rcode = mkrs((const void*)(pc & ~(u64)255), CODE_BYTES);
      const u32 c0 = __builtin_amdgcn_raw_buffer_load_b32(rcode, lane * 128, 0, 0);
      asm volatile("" ::"v"(c0));
    }
    *(LAS u32*)(L + L_FLAG + 4) = tag;
    const u32 tag4 = __builtin_amdgcn_perm(tag, tag, 0u);
    STAMP(1);
    {
      u32 t[9];
#pragma unroll 1
      for (u32 spin = 0; spin < SPIN_CAP * 8; ++spin) {
#pragma unroll
        for (int k = 0; k < 9; ++k) t[k] = *(volatile LAS u32*)(L + L_QP + k * 256 + lane * 4);
        if (none(max(max(max(max(t[0], t[1]), max(t[2], t[3])), max(max(t[4], t[5]), max(t[6], t[7]))), t[8]) == SENT)) break;
        __builtin_amdgcn_s_sleep(1);
      }
      const float qp = ((asf(t[0]) + asf(t[1])) + (asf(t[2]) + asf(t[3]))) + ((asf(t[4]) + asf(t[5])) + (asf(t[6]) + asf(t[7]))) + asf(t[8]);
      __builtin_amdgcn_raw_buffer_store_b64(u32x2{__float_as_uint(qp), tag}, rs, WS_Q + ((rbr * 2 + rh) * 64 + cb) * 512 + lane * 8, 0, SC1);
    }
    STAMP(2);
    {
      u32x4 v[8];
      u32 spin = 0;
#pragma unroll 1
      for (; spin < SPIN_CAP; ++spin) {
        __builtin_amdgcn_s_sleep(QSLEEP);
        asm volatile("" ::: "memory");
        u32 bad = 0;
#pragma unroll
        for (int i = 0; i < 8; ++i) v[i] = ldsc(rs, WS_Q + h * 2048 + lane * 16 + ((i >> 1) & 1) * 1024, ((i >> 2) * 2 + (i & 1)) * 32768);
#pragma unroll
        for (int i = 0; i < 8; ++i) bad |= badtag(v[i], tag);
        if (none(bad != 0)) break;
      }
      STAMPV(22, spin + 1);
#pragma unroll
      for (int i = 0; i < 4; ++i)
        *(LAS u32*)(L + L_Q + i * 256 + lane * 4) = cvtpk(asf(v[2 * i][0]) + asf(v[2 * i + 1][0]), asf(v[2 * i][2]) + asf(v[2 * i + 1][2]));
      LGKWAIT();
      *(volatile LAS u32*)(L + L_FLAG) = 0;
    }
    STAMP(4);
    {
      u32x2 ml;
#pragma unroll 1
      for (u32 spin = 0; spin < SPIN_CAP * 8; ++spin) {
        ml = *(volatile LAS u32x2*)(L + L_ML + (lane < 20 ? lane : 0) * 8);
        if (none(max(ml[0], ml[1]) == SENT)) break;
        __builtin_amdgcn_s_sleep(2);
      }
      STAMP(6);
      const bool live = lane < 20;
      float m = live ? asf(ml[0]) : -INFINITY;
      float M = fmaxf(m, dpp<XOR2>(m)); M = fmaxf(M, dpp<ROR4>(M)); M = fmaxf(M, dpp<ROR8>(M));
      const float F = __builtin_amdgcn_exp2f(m - M);
      if (live) *(LAS float*)(L + L_F + lane * 4) = F;
      float l = live ? asf(ml[1]) * F : 0.f;
      l += dpp<XOR2>(l); l += dpp<ROR4>(l); l += dpp<ROR8>(l);
      __builtin_amdgcn_raw_buffer_store_b128(u32x4{__float_as_uint(M), tag, __float_as_uint(l), tag}, rs,
                                             (lane & 46) == 0 ? WS_ML + h * 1024 + ((lane < 16 ? 2 : 0) + (lane & 1)) * 256 + s * 16 : WS_END, 0, SC1);
    }
    const int moff = WS_ML + hc * 1024 + (rbr * 2 + (rc >> 2)) * 256 + (lane & 15) * 16;
    const int ioff = WS_O + (hc * 16 + rbr * 8 + rc) * 2048 + (lane & 7) * 128 + (lane >> 3) * 16;
    const int rr = ((j >> 2) * 4 + (x & 3)) * 4 + (j & 3), br2 = rbr ^ 1;
    const int yoff = WS_Y + (br2 * 128 + (lane >> 1)) * 2304 + rr * 32 + (lane & 1) * 16;
    const int outoff = (lane & 14) == 0 ? (br2 * 576 + rr * 8 + 4 * (lane & 1) + (lane >> 4)) * 2 : 4096;
    asm volatile("" ::"v"(yoff), "v"(ioff), "v"(moff), "v"(outoff));
    STAMP(7);
#pragma unroll 1
    for (u32 spin = 0; spin < SPIN_CAP * 8; ++spin) {
      if (*(volatile LAS u32*)(L + L_FLAG + 8) != SENT) break;
      __builtin_amdgcn_s_sleep(1);
    }
    STAMP(8);
    float w0, w1;
    {
      u32x4 gv;
#pragma unroll 1
      for (u32 spin = 0; spin < SPIN_CAP; ++spin) {
        asm volatile("" ::: "memory");
        gv = ldsc(rs, moff, 0);
        if (none(badtag(gv, tag) != 0)) break;
      }
      const float gm = asf(gv[0]);
      float GM = fmaxf(gm, dpp<XOR1>(gm)); GM = fmaxf(GM, dpp<XOR2>(GM)); GM = fmaxf(GM, dpp<HMIR>(GM)); GM = fmaxf(GM, dpp<ROR8>(GM));
      const float e = __builtin_amdgcn_exp2f(gm - GM);
      float GL = asf(gv[2]) * e;
      GL += dpp<XOR1>(GL); GL += dpp<XOR2>(GL); GL += dpp<HMIR>(GL); GL += dpp<ROR8>(GL);
      const float w = e / GL, wx = dpp<ROR8>(w);
      w0 = (lane & 8) ? wx : w; w1 = (lane & 8) ? w : wx;
      asm volatile("" ::"v"(w0), "v"(w1));
    }
#pragma unroll 1
    for (u32 spin = 0; spin < SPIN_CAP * 8; ++spin) {
      const u32x2 c = *(volatile LAS u32x2*)(L + L_FLAG + 8);
      if (c[0] == 7u && c[1] == 1u) break;
    }
    STAMP(9);
    {
      u32x4 v0, v1;
      u32 spin = 0;
#pragma unroll 1
      for (; spin < SPIN_CAP; ++spin) {
        asm volatile("" ::: "memory");
        v0 = ldsc(rs, ioff, 0);
        v1 = ldsc(rs, ioff + 1024, 0);
        if (none(((twtags(v0) ^ tag4) | (twtags(v1) ^ tag4)) != 0)) break;
      }
      STAMP(10); STAMPV(20, spin + 1);
      float ov[4];
#pragma unroll
      for (int i = 0; i < 4; ++i) ov[i] = twval(v0[i]) * w0 + twval(v1[i]) * w1;
      SB();
#pragma unroll
      for (int i = 0; i < 4; ++i) ov[i] += dpp<XOR1>(ov[i]);
      SB();
#pragma unroll
      for (int i = 0; i < 4; ++i) ov[i] += dpp<XOR2>(ov[i]);
      SB();
#pragma unroll
      for (int i = 0; i < 4; ++i) ov[i] += dpp<HMIR>(ov[i]);
      SB();
      const u32x4 e = {cvtpk(ov[0], ov[0]), cvtpk(ov[1], ov[1]), cvtpk(ov[2], ov[2]), cvtpk(ov[3], ov[3])};
      *(LAS u32x4*)(L + L_OF + (lane >> 3) * 16) = e;
    }
    BARRIER();
    STAMP(11);
    if (rr < 72) {
      u32x4 v[4];
      u32 spin = 0;
#pragma unroll 1
      for (; spin < SPIN_CAP; ++spin) {
        asm volatile("" ::: "memory");
#pragma unroll
        for (int i = 0; i < 4; ++i) v[i] = ldsc(rs, yoff, i * 32 * 2304);
        if (none((((twtags(v[0]) ^ tag4) | (twtags(v[1]) ^ tag4)) | ((twtags(v[2]) ^ tag4) | (twtags(v[3]) ^ tag4))) != 0)) break;
      }
      STAMP(12); STAMPV(21, spin + 1);
      float y[4];
#pragma unroll
      for (int k = 0; k < 4; ++k) y[k] = (twval(v[0][k]) + twval(v[1][k])) + (twval(v[2][k]) + twval(v[3][k]));
      auto t0 = __builtin_amdgcn_permlane32_swap(__float_as_uint(y[0]), __float_as_uint(y[2]), false, false);
      auto t1 = __builtin_amdgcn_permlane32_swap(__float_as_uint(y[1]), __float_as_uint(y[3]), false, false);
      const float z0 = asf(t0[0]) + asf(t0[1]), z1 = asf(t1[0]) + asf(t1[1]);
      auto t2 = __builtin_amdgcn_permlane16_swap(__float_as_uint(z0), __float_as_uint(z1), false, false);
      float z = asf(t2[0]) + asf(t2[1]);
      z += dpp<ROR8>(z); z += dpp<ROR4>(z); z += dpp<XOR2>(z);
      __builtin_amdgcn_raw_buffer_store_b16((unsigned short)cvtpk(z, z), mkrs(out_, 2304), outoff, 0, SC1);
      if (b == 0) __builtin_amdgcn_raw_buffer_store_b32(tag, rs, lane == 0 ? WS_EPOCH : WS_END, 0, SC1);
    }
    STAMP(13);
    FINISH();
  }
  const u32 kvo = (u32)(h * 8192 + (br ? s * 512 + a * 4 : 6144 + s * 128 + (a - 8) * 4)) * 256;
  const u32 kvb = br ? 131072 : 32768, sh = br ? 13 : 11;
  const rsrc_t rk = mkrs((br ? kB : kA) + kvo, kvb);
  const rsrc_t rv = mkrs((br ? vB : vA) + kvo, kvb);
  const u32 koff = (u32)((lane >> 2) & 3) * 256 + (lane & 3) * 64 + (lane >> 4) * 16, voff = lane * 16;
#define CHUNK(j_) ((u32)((j_) ^ s) << sh)
  u32x4 msk;
#pragma unroll
  for (int k = 0; k < 4; ++k) msk[k] = ((lane & 7) >> 1) == k ? ((lane & 1) ? 0xffff0000u : 0x0000ffffu) : 0u;
  const int colp = (lane & 7) * 8 + ((lane >> 4) & 1) * 4 + (lane >> 5) * 2;
  SB();
#pragma unroll
  for (int i = WIN; i < 4 + WIN; ++i) { VMWAIT(WIN - 1); if (i < 4) V[i] = ldnt(rq, qoff, i * 8192); else K[i - 4] = ldnt(rk, koff, CHUNK(i - 4)); SB(); }
  SB();
  {
    const u32x4 e = {__builtin_amdgcn_perm(xv[0], xv[0], 0x01000100u), __builtin_amdgcn_perm(xv[0], xv[0], 0x03020302u),
                     __builtin_amdgcn_perm(xv[1], xv[1], 0x01000100u), __builtin_amdgcn_perm(xv[1], xv[1], 0x03020302u)};
    const f32x2 qp = gemv_tile(e, msk, V);
    if (!(lane & 8) && a < 9) *(LAS f32x2*)(L + L_QP + a * 256 + colp * 4) = qp;
  }
  SB();
  STAMP(1);
#pragma unroll
  for (int i = WIN; i < 16; ++i) {
    VMWAIT(WIN - 1);
    K[i] = ldnt(rk, koff, CHUNK(i));
    SB();
  }
  const rsrc_t ro = mkrs((rbr ? woB : woA) + (u32)(hc * 256 + rc * 32) * 1152, a < 9 ? 32 * 1152 : 0);
  const u32 wooff = (u32)(4 * (lane >> 3)) * 1152 + (lane & 7) * 16 + a * 128;
#define ISSUE(n) do { if ((n) < 16) V[n] = ldnt(rv, voff, CHUNK(n)); else if ((n) < 20) V[n] = ldnt(ro, wooff + ((n) - 16) * 1152, 0); } while (0)
  float sc[16];
  u32 P[16];
  u32x4 Q;
  u32 tag;
  float m, l = 0.f, F;
  const float LOG2E = 1.4426950408889634f;
  const int yo = (lane & 8) ? WS_END : WS_Y + (rbr * 128 + hc * 8 + rc) * 2304 + (a * 64 + colp) * 4;
  const int ooff = WS_O + (h * 16 + br * 8 + (lane >> 5) * 4 + ((lane & 15) >> 2)) * 2048 + s * 128 + (lane & 3) * 32 + ((lane >> 4) & 1) * 16;
  LAS unsigned char* cntp = L + L_FLAG + 12 - br * 4;
  f32x4 o = {0.f, 0.f, 0.f, 0.f};
  sfor<0, 16 + WIN>([&](auto nc) {
    constexpr int n = decltype(nc)::v;
    u32x4 sel;
    if constexpr (n > QOFF + 6) { sel = msk & P[n - WIN]; asm volatile("" : "+v"(sel)); }
    SB();
    VMWAIT(issued_after(n - 1) - (n - WIN + 1));
    sfor<issued_after(n - 1), issued_after(n)>([&](auto kc) { constexpr int k = decltype(kc)::v; ISSUE(k); });
    SB();
    if constexpr (n == QOFF) {
      STAMP(2);
#pragma unroll 1
      for (u32 spin = 0; spin < SPIN_CAP * 8; ++spin) { if (*(volatile LAS u32*)(L + L_FLAG) == 0) break; __builtin_amdgcn_s_sleep(1); }
      asm volatile("" ::: "memory");
      STAMP(3);
      Q = *(LAS u32x4*)(L + L_Q + br * 512 + ((lane >> 3) & 1) * 256 + (lane & 3) * 64 + (lane >> 4) * 16);
      tag = *(LAS u32*)(L + L_FLAG + 4);
    }
    if constexpr (n < QOFF) {
    } else if constexpr (n < QOFF + 4) {
      constexpr int T = n - QOFF;
      float d[4];
#pragma unroll
      for (int u = 0; u < 4; ++u) {
        const f32x4 t = mfma(K[T * 4 + u], Q, f32x4{0.f, 0.f, 0.f, 0.f});
        d[u] = ((dpp<0x00>(t[0]) + dpp<0x55>(t[1])) + dpp<0xAA>(t[2])) + dpp<0xFF>(t[3]);
      }
      const u32 r0 = cvtpk(d[0], d[1]), r1 = cvtpk(d[2], d[3]);
      const float SC = 0.08838834764831845f;
      const u32 r2 = cvtpk(bflo(r0) * SC, bfhi(r0) * SC), r3 = cvtpk(bflo(r1) * SC, bfhi(r1) * SC);
      sc[T * 4 + 0] = bflo(r2); sc[T * 4 + 1] = bfhi(r2); sc[T * 4 + 2] = bflo(r3); sc[T * 4 + 3] = bfhi(r3);
    } else if constexpr (n == QOFF + 4) {
      m = sc[0];
#pragma unroll
      for (int i = 1; i < 16; ++i) m = fmaxf(m, sc[i]);
      m = x16_32_max(m);
#pragma unroll
      for (int i = 0; i < 6; ++i) { const float p = __builtin_amdgcn_exp2f((sc[i] - m) * LOG2E); l += p; P[i] = cvtpk(p, p); }
    } else if constexpr (n == QOFF + 5) {
#pragma unroll
      for (int i = 6; i < 16; ++i) { const float p = __builtin_amdgcn_exp2f((sc[i] - m) * LOG2E); l += p; P[i] = cvtpk(p, p); }
      l = x16_32_sum(l);
      if ((lane & 55) == 0) *(LAS f32x2*)(L + L_ML + a * 16 + (lane >> 3) * 8) = f32x2{m * LOG2E, l};
      asm volatile("" ::"v"(yo), "v"(ooff), "v"(cntp));
      STAMP(4);
    } else if constexpr (n == QOFF + 6) {
#pragma unroll
      for (int i = 0; i <= n - WIN; ++i) o = mfma(msk & P[i], V[i], o);
    } else {
      o = mfma(sel, V[n - WIN], o);
      asm volatile("" : "+v"(o));
      if constexpr (n == 15) {
#pragma unroll 1
        for (u32 spin = 0; spin < SPIN_CAP * 8; ++spin) {
          const u32 f = *(volatile LAS u32*)(L + L_F + a * 8 + (lane >> 5) * 4);
          F = __uint_as_float(f);
          if (none(f == SENT)) break;
          __builtin_amdgcn_s_sleep(1);
        }
      }
    }
    SB();
  });
#pragma unroll
  for (int k = issued_after(99); k < 20; ++k) ISSUE(k);
  SB();
  *(LAS f32x4*)(L + L_O + a * 1024 + lane * 16) = o * F;
  u32 arr;
  asm volatile("s_mov_b64 exec, 1\n\tds_add_rtn_u32 %0, %1, %2\n\ts_mov_b64 exec, -1\n\ts_waitcnt lgkmcnt(0)"
               : "=&v"(arr) : "v"(cntp), "v"(1u) : "memory");
  arr = __builtin_amdgcn_readfirstlane(arr);
  STAMP(5);
  if (arr == (br ? 6u : 0u)) {
    f32x4 t;
    if (br) {
      f32x4 u[8];
#pragma unroll
      for (int k = 0; k < 8; ++k) u[k] = *(LAS f32x4*)(L + L_O + k * 1024 + lane * 16);
      t = ((u[0] + u[1]) + (u[2] + u[3])) + ((u[4] + u[5]) + (u[6] + u[7]));
    } else {
      t = *(LAS f32x4*)(L + L_O + 8 * 1024 + lane * 16) + *(LAS f32x4*)(L + L_O + 9 * 1024 + lane * 16);
    }
    __builtin_amdgcn_raw_buffer_store_b128(u32x4{tw(t[0], tag), tw(t[1], tag), tw(t[2], tag), tw(t[3], tag)}, rs, ooff, 0, SC1);
    STAMP(8);
  }
  if (a == 9) FINISH();
  BARRIER();
  const u32x4 e = *(LAS u32x4*)(L + L_OF + (lane >> 3) * 16);
  const f32x2 y = gemv_tile(e, msk, V + 16);
  __builtin_amdgcn_raw_buffer_store_b64(u32x2{tw(y[0], tag), tw(y[1], tag)}, rs, yo, 0, SC1);
  STAMP(7);
  FINISH();
}

extern "C" void kernel_launch(void* const* d_in, const int* in_sizes, int n_in, void* d_out, int out_size, void* d_ws,
                              size_t ws_size, hipStream_t stream) {
  typedef const unsigned char* P;
  fused_decode<<<256, NTHR, 0, stream>>>((P)d_in[0], (P)d_in[1], (P)d_in[2], (P)d_in[3], (P)d_in[4], (P)d_in[5], (P)d_in[6],
                                         (P)d_in[7], (P)d_in[8], (P)d_in[9], (unsigned char*)d_out, (unsigned char*)d_ws);
}
```

```cpp
#define STARTM 1
#define QOFF 1
#define WIN 4
#define TSYNC 1
#define STAG 1
#define PACE 355
#define PT0 -10
#define DS 0
#define DW 0
#define DK 0
#define DM 10
#define DV 0
#include <hip/hip_runtime.h>
#include <hip/hip_bf16.h>
#include <stdint.h>

#define STAMPS 0
#define CODEPF 1
#define QSLEEP 16
#define WOM 0
#define MLSLEEP 48
#define WOCNT 10
#define POLLCNT 10
#define PACEN 16
#define WOPRE 1
#define WOPACE 1
#define RED 0
#define OUTPF 0
#define OAUX 16
#define WEND WIN

typedef unsigned int u32;
typedef unsigned long long u64;
typedef u32 u32x2 __attribute__((ext_vector_type(2)));
typedef u32 u32x4 __attribute__((ext_vector_type(4)));
typedef float f32x2 __attribute__((ext_vector_type(2)));
typedef float f32x4 __attribute__((ext_vector_type(4)));
typedef __bf16 bf16x2 __attribute__((ext_vector_type(2)));
typedef __bf16 bf16x8 __attribute__((ext_vector_type(8)));
#define GAS __attribute__((address_space(1)))
#define LAS __attribute__((address_space(3)))
typedef __amdgpu_buffer_rsrc_t rsrc_t;

constexpr int NWAVE = 11, NTHR = NWAVE * 64;
constexpr u32 SENT = 0xFFFFFFFFu;
constexpr u32 SPIN_CAP = 1u << 17;
constexpr int WS_EPOCH = 0;
constexpr int WS_Q = 4096;
constexpr int WS_ML = WS_Q + 256 * 512;
constexpr int WS_O = WS_ML + 256 * 128;
constexpr int WS_Y = WS_O + 256 * 2048;
constexpr int WS_END = WS_Y + 256 * 2304;
constexpr int WS_STAMP = 4 << 20;
constexpr int L_QP = 0;
constexpr int L_ML = 2304;
constexpr int L_F = L_ML + 160;
constexpr int L_FLAG = L_F + 80;
constexpr int L_OF = 2816;
constexpr int L_Q = 3072;
constexpr int L_O = 4096;
constexpr int L_BYTES = L_O + 10 * 1024;
constexpr int SC1 = 16, NT = 2;
#define CODEB 9040
constexpr u32 CODE_BYTES = CODEB;

__device__ __forceinline__ rsrc_t mkrs(const void* p, u32 bytes) { return __builtin_amdgcn_make_buffer_rsrc((void*)p, 0, bytes, 0x00020000); }
__device__ __forceinline__ u32x4 ldnt(rsrc_t r, u32 voff, u32 soff) { return __builtin_amdgcn_raw_buffer_load_b128(r, voff, soff, NT); }
__device__ __forceinline__ u32x4 ldsc(rsrc_t r, u32 voff, u32 soff) { return __builtin_amdgcn_raw_buffer_load_b128(r, voff, soff, SC1); }
__device__ __forceinline__ u32 badtag(u32x4 v, u32 tag) { return (v[1] ^ tag) | (v[3] ^ tag); }
__device__ __forceinline__ u32 tw(float x, u32 tag) { return __builtin_amdgcn_perm(__float_as_uint(x), tag, 0x07060500u); }
__device__ __forceinline__ float twval(u32 w) { return __uint_as_float(w & 0xffffff00u); }
__device__ __forceinline__ u32 twtags(u32x4 v) {
  return __builtin_amdgcn_perm(__builtin_amdgcn_perm(v[3], v[2], 0x0c0c0400u), __builtin_amdgcn_perm(v[1], v[0], 0x0c0c0400u), 0x05040100u);
}
__device__ __forceinline__ f32x4 mfma(u32x4 a, u32x4 b, f32x4 c) {
  return __builtin_amdgcn_mfma_f32_16x16x32_bf16(__builtin_bit_cast(bf16x8, a), __builtin_bit_cast(bf16x8, b), c, 0, 0, 0);
}
__device__ __forceinline__ u32 cvtpk(float lo, float hi) {
  bf16x2 r = {(__bf16)lo, (__bf16)hi};
  return __builtin_bit_cast(u32, r);
}
__device__ __forceinline__ float bflo(u32 x) { return __uint_as_float(x << 16); }
__device__ __forceinline__ float bfhi(u32 x) { return __uint_as_float(x & 0xffff0000u); }
__device__ __forceinline__ float asf(u32 x) { return __uint_as_float(x); }
template <int CTRL> __device__ __forceinline__ float dpp(float x) {
  return __builtin_bit_cast(float, __builtin_amdgcn_mov_dpp(__builtin_bit_cast(int, x), CTRL, 0xf, 0xf, true));
}
constexpr int XOR1 = 0xB1, XOR2 = 0x4E, HMIR = 0x141, ROR8 = 0x128, ROR4 = 0x124;
__device__ __forceinline__ float x16_32_max(float x) {
  auto s = __builtin_amdgcn_permlane16_swap(__float_as_uint(x), __float_as_uint(x), false, false);
  x = fmaxf(__uint_as_float(s[0]), __uint_as_float(s[1]));
  auto t = __builtin_amdgcn_permlane32_swap(__float_as_uint(x), __float_as_uint(x), false, false);
  return fmaxf(__uint_as_float(t[0]), __uint_as_float(t[1]));
}
__device__ __forceinline__ float x16_32_sum(float x) {
  auto s = __builtin_amdgcn_permlane16_swap(__float_as_uint(x), __float_as_uint(x), false, false);
  x = __uint_as_float(s[0]) + __uint_as_float(s[1]);
  auto t = __builtin_amdgcn_permlane32_swap(__float_as_uint(x), __float_as_uint(x), false, false);
  return __uint_as_float(t[0]) + __uint_as_float(t[1]);
}
__device__ __forceinline__ bool none(bool p) { return __builtin_amdgcn_ballot_w64(p) == 0; }
__device__ __forceinline__ f32x2 gemv_tile(u32x4 e, u32x4 msk, const u32x4* w) {
  f32x4 y = {0.f, 0.f, 0.f, 0.f};
#pragma unroll
  for (int i = 0; i < 4; ++i) y = mfma(msk & e[i], w[i], y);
  auto t0 = __builtin_amdgcn_permlane32_swap(__float_as_uint(y[0]), __float_as_uint(y[2]), false, false);
  auto t1 = __builtin_amdgcn_permlane32_swap(__float_as_uint(y[1]), __float_as_uint(y[3]), false, false);
  return f32x2{__uint_as_float(t0[0]) + dpp<ROR8>(__uint_as_float(t0[1])), __uint_as_float(t1[0]) + dpp<ROR8>(__uint_as_float(t1[1]))};
}
template <int I> struct IC { static constexpr int v = I; };
template <int LO, int HI, class Fn> __device__ __forceinline__ void sfor(Fn&& f) {
  if constexpr (LO < HI) { f(IC<LO>{}); sfor<LO + 1, HI>(f); }
}
constexpr int issued_after(int n) { return n < 0 ? 0 : (n + 1 < 16 + WOPRE ? n + 1 : 16 + WOPRE); }
#define SB() __builtin_amdgcn_sched_barrier(0)
#define VMWAIT(n) asm volatile("s_waitcnt vmcnt(%0)" ::"n"(n) : "memory")
#define LGKWAIT() asm volatile("s_waitcnt lgkmcnt(0)" ::: "memory")
#define BARRIER() asm volatile("s_waitcnt lgkmcnt(0)\n\ts_barrier" ::: "memory")
#define BAR() asm volatile("s_barrier" ::: "memory")
template <int G> __device__ __forceinline__ void pace(u32 t0) {
  asm volatile("1:\n\ts_memrealtime vcc\n\ts_waitcnt lgkmcnt(0)\n\ts_sub_u32 vcc_lo, vcc_lo, %0\n\ts_cmp_lt_i32 vcc_lo, %1\n\ts_cbranch_scc1 1b"
               : : "s"(t0), "n"(PT0 + G * PACE / 10) : "memory", "scc", "vcc");
}
#define RT(x) asm volatile("s_memrealtime %0\n\ts_waitcnt lgkmcnt(0)" : "=s"(x)::"memory")
#define STAMP(i) do { if ((i) == DS && wave == DW) { if (DV) VMWAIT(0); RT(tdiag); } } while (0)
#define STAMPV(i, v) do {} while (0)
#define FINISH() do { GAS u64* sp_ = (GAS u64*)((GAS unsigned char*)ws_ + (6 << 20)); \
  if (wave == DW && lane == 0) { __hip_atomic_store(sp_ + b, tstart, __ATOMIC_RELAXED, __HIP_MEMORY_SCOPE_AGENT); __hip_atomic_store(sp_ + 256 + b, tdiag, __ATOMIC_RELAXED, __HIP_MEMORY_SCOPE_AGENT); } \
  if (wave == 0 && b == 0) { u64 t; do { __builtin_amdgcn_s_sleep(4); RT(t); } while (t < tstart + 3500); \
    u64 mn = ~0ull, mx = 0, dn = ~0ull, sum = 0; for (int i = 0; i < 4; ++i) { u64 v = __hip_atomic_load(sp_ + i * 64 + lane, __ATOMIC_RELAXED, __HIP_MEMORY_SCOPE_AGENT); u64 d = __hip_atomic_load(sp_ + 256 + i * 64 + lane, __ATOMIC_RELAXED, __HIP_MEMORY_SCOPE_AGENT); mn = v < mn ? v : mn; mx = d > mx ? d : mx; dn = d < dn ? d : dn; sum += d; } \
    for (int m_ = 1; m_ < 64; m_ <<= 1) { u64 o1 = __shfl_xor(mn, m_), o2 = __shfl_xor(mx, m_), o3 = __shfl_xor(dn, m_), o4 = __shfl_xor(sum, m_); mn = o1 < mn ? o1 : mn; mx = o2 > mx ? o2 : mx; dn = o3 < dn ? o3 : dn; sum += o4; } \
    u64 xv_ = DK == 0 ? mx - mn : DK == 1 ? dn - mn : sum / 256 - mn; if (xv_ > 4000) xv_ = 4000; \
    do { __builtin_amdgcn_s_sleep(4); RT(t); } while (t < mn + 5000 + DM * xv_); } __builtin_amdgcn_endpgm(); } while (0)

__global__ void __launch_bounds__(NTHR, 3)
fused_decode(const unsigned char* xA, const unsigned char* kA, const unsigned char* vA, const unsigned char* wqA,
             const unsigned char* woA, const unsigned char* xB, const unsigned char* kB, const unsigned char* vB,
             const unsigned char* wqB, const unsigned char* woB, unsigned char* out_, unsigned char* ws_) {
  __shared__ __attribute__((aligned(16))) unsigned char smem[L_BYTES];
  LAS unsigned char* L = (LAS unsigned char*)smem;
  u64 pc;
  asm volatile("s_getpc_b64 %0" : "=s"(pc));
  const int b = blockIdx.x, lane = threadIdx.x & 63;
  const int wave = __builtin_amdgcn_readfirstlane(threadIdx.x >> 6);
  const int x = b & 7, j = b >> 3;
#define HEAD_OF(x_, k_) ((((x_) & 1) ^ 1) * 8 + ((x_) >> 1) * 2 + (k_))
  const int h = HEAD_OF(x, j >> 4), s = j & 15;
  const int rbr = x >> 2, rh = (x >> 1) & 1, cb = (x & 1) * 32 + j;
  const int xs = x ^ (1 + 3 * (j >> 4));
  const int hc = HEAD_OF(xs, (j >> 3) & 1), rc = j & 7;
  const rsrc_t rs = mkrs(ws_, WS_END);
  u64 tstart, tdiag; RT(tstart); tdiag = tstart;
  u32 t0 = PACE ? (u32)__builtin_amdgcn_s_memrealtime() : 0u;
  *(LAS u32*)(L + threadIdx.x * 4) = SENT;
  BARRIER();
  asm volatile("" ::"s"(xA), "s"(kA), "s"(vA), "s"(wqA), "s"(woA), "s"(xB), "s"(kB), "s"(vB), "s"(wqB), "s"(woB), "s"(out_), "s"(ws_));
  const int a = wave - 1, br = a < 8;
  u32x4 K[16], V[20];
  const int row0 = rh * 288 + a * 32;
  const rsrc_t rq = mkrs(rbr ? wqB : wqA, (u32)a < 9u ? 576 * 8192 : 0);
  const u32 qoff = (u32)(row0 + 4 * (lane >> 3)) * 8192 + cb * 128 + (lane & 7) * 16;
  const u32x2 xv = __builtin_amdgcn_raw_buffer_load_b64(mkrs(rbr ? xB : xA, (u32)a < 9u ? 1152 : 0), (row0 + 4 * (lane >> 3)) * 2, 0, 0);
  const u32 kvo = (u32)(h * 8192 + (br ? s * 512 + a * 4 : 6144 + s * 128 + (a - 8) * 4)) * 256;
  const u32 kvb = wave == 0 ? 0 : (br ? 131072 : 32768), sh = br ? 13 : 11;
  const rsrc_t rk = mkrs((br ? kB : kA) + kvo, kvb);
  const u32 koff = (u32)((lane >> 2) & 3) * 256 + (lane & 3) * 64 + (lane >> 4) * 16, voff = lane * 16;
#define CHUNK(j_) ((u32)((j_) ^ s) << sh)
#define P1_ISWQ(g) (STARTM == 2 ? ((g) < 8 && !((g) & 1)) : (g) < 4)
#define P1_IDX(g) (STARTM == 2 ? ((g) < 8 ? (g) >> 1 : (g) - 4) : ((g) < 4 ? (g) : (g) - 4))
#define ISSUE1(g) do { if constexpr (P1_ISWQ(g)) V[P1_IDX(g)] = ldnt(rq, qoff, P1_IDX(g) * 8192); else K[P1_IDX(g)] = ldnt(rk, koff, CHUNK(P1_IDX(g))); } while (0)
  sfor<0, WIN>([&](auto gc) { constexpr int g = decltype(gc)::v; if constexpr (g > 0) BAR(); ISSUE1(g); });
  SB();
  if (wave == 0) {
    const u32 tag = __builtin_amdgcn_raw_buffer_load_b32(rs, WS_EPOCH, 0, SC1) + 1u;
    {
      const rsrc_t rcode = mkrs((const void*)(pc & ~(u64)255), CODE_BYTES);
      const u32 c0 = __builtin_amdgcn_raw_buffer_load_b32(rcode, lane * 128, 0, 0);
      const u32 c1 = __builtin_amdgcn_raw_buffer_load_b32(rcode, lane * 128, 8192, 0);
      asm volatile("" ::"v"(c0), "v"(c1));
    }
    *(LAS u32*)(L + L_FLAG + 4) = tag;
    const u32 tag4 = __builtin_amdgcn_perm(tag, tag, 0u);
    STAMP(1);
    {
      u32 t[9];
#pragma unroll 1
      for (u32 spin = 0; spin < SPIN_CAP * 8; ++spin) {
#pragma unroll
        for (int k = 0; k < 9; ++k) t[k] = *(volatile LAS u32*)(L + L_QP + k * 256 + lane * 4);
        if (none(max(max(max(max(t[0], t[1]), max(t[2], t[3])), max(max(t[4], t[5]), max(t[6], t[7]))), t[8]) == SENT)) break;
        __builtin_amdgcn_s_sleep(1);
      }
      const float qp = ((asf(t[0]) + asf(t[1])) + (asf(t[2]) + asf(t[3]))) + ((asf(t[4]) + asf(t[5])) + (asf(t[6]) + asf(t[7]))) + asf(t[8]);
      __builtin_amdgcn_raw_buffer_store_b64(u32x2{__float_as_uint(qp), TSYNC ? (tag & 0xffffu) | (t0 << 16) : tag}, rs, WS_Q + ((rbr * 2 + rh) * 64 + cb) * 512 + lane * 8, 0, SC1);
    }
    STAMP(2);
    {
      u32x4 v[8];
      u32 spin = 0;
#pragma unroll 1
      for (; spin < SPIN_CAP; ++spin) {
        __builtin_amdgcn_s_sleep(QSLEEP);
        asm volatile("" ::: "memory");
        u32 bad = 0;
#pragma unroll
        for (int i = 0; i < 8; ++i) v[i] = ldsc(rs, WS_Q + h * 2048 + lane * 16 + ((i >> 1) & 1) * 1024, ((i >> 2) * 2 + (i & 1)) * 32768);
#pragma unroll
        for (int i = 0; i < 8; ++i) bad |= badtag(v[i], tag);
        if (TSYNC) bad &= 0xffffu;
        if (none(bad != 0)) break;
      }
      STAMPV(22, spin + 1);
      {
        const int d = (((int)(v[0][1] - (t0 << 16)) >> 16) + ((int)(v[1][1] - (t0 << 16)) >> 16) + ((int)(v[4][1] - (t0 << 16)) >> 16) + ((int)(v[5][1] - (t0 << 16)) >> 16)) >> 2;
        *(LAS int*)(L + L_FLAG + 16) = __builtin_amdgcn_readfirstlane(d);
      }
#pragma unroll
      for (int i = 0; i < 4; ++i)
        *(LAS u32*)(L + L_Q + i * 256 + lane * 4) = cvtpk(asf(v[2 * i][0]) + asf(v[2 * i + 1][0]), asf(v[2 * i][2]) + asf(v[2 * i + 1][2]));
      LGKWAIT();
      *(volatile LAS u32*)(L + L_FLAG) = 0;
    }
    STAMP(4);
    {
      u32x2 ml;
#pragma unroll 1
      for (u32 spin = 0; spin < SPIN_CAP * 8; ++spin) {
        ml = *(volatile LAS u32x2*)(L + L_ML + (lane < 20 ? lane : 0) * 8);
        if (none(max(ml[0], ml[1]) == SENT)) break;
        __builtin_amdgcn_s_sleep(2);
      }
      STAMP(6);
      const bool live = lane < 20;
      float m = live ? asf(ml[0]) : -INFINITY;
      float M = fmaxf(m, dpp<XOR2>(m)); M = fmaxf(M, dpp<ROR4>(M)); M = fmaxf(M, dpp<ROR8>(M));
      const float F = __builtin_amdgcn_exp2f(m - M);
      if (live) *(LAS float*)(L + L_F + lane * 4) = F;
      float l = live ? asf(ml[1]) * F : 0.f;
      l += dpp<XOR2>(l); l += dpp<ROR4>(l); l += dpp<ROR8>(l);
      __builtin_amdgcn_raw_buffer_store_b128(u32x4{__float_as_uint(M), tag, __float_as_uint(l), tag}, rs,
                                             (lane & 46) == 0 ? WS_ML + (h * 16 + s) * 128 + ((lane < 16 ? 2 : 0) + (lane & 1)) * 16 : WS_END, 0, SC1);
    }
    const int moff = WS_ML + (hc * 16 + (lane & 15)) * 128 + (rbr * 2 + (rc >> 2)) * 16;
    const int ioff = WS_O + (hc * 16 + rbr * 8 + rc) * 2048 + (lane & 7) * 128 + (lane >> 3) * 16;
    const int br2 = rbr ^ 1;
    const int rr = ((j >> 2) * 4 + (x & 3)) * 4 + (j & 3);
    constexpr int NRED = 72;
    const int yoff = WS_Y + (br2 * 128 + (lane >> 1)) * 2304 + rr * 32 + (lane & 1) * 16;
    const int outoff = (lane & 14) == 0 ? (br2 * 576 + rr * 8 + 4 * (lane & 1) + (lane >> 4)) * 2 : 4096;
    asm volatile("" ::"v"(yoff), "v"(ioff), "v"(moff), "v"(outoff));
    STAMP(7);
#pragma unroll 1
    for (u32 spin = 0; spin < SPIN_CAP * 8; ++spin) {
      const u32x2 c = *(volatile LAS u32x2*)(L + L_FLAG + 8);
      if ((int)(c[0] + c[1]) >= POLLCNT - 2) break;
      __builtin_amdgcn_s_sleep(1);
    }
    STAMP(8);
    {
      u32x4 v0, v1, gv;
      u32 spin = 0;
#pragma unroll 1
      for (; spin < SPIN_CAP; ++spin) {
        asm volatile("" ::: "memory");
        gv = ldsc(rs, moff, 0);
        v0 = ldsc(rs, ioff, 0);
        v1 = ldsc(rs, ioff + 1024, 0);
        if (none(badtag(gv, tag) != 0)) break;
      }
      const float gm = asf(gv[0]);
      float GM = fmaxf(gm, dpp<XOR1>(gm)); GM = fmaxf(GM, dpp<XOR2>(GM)); GM = fmaxf(GM, dpp<HMIR>(GM)); GM = fmaxf(GM, dpp<ROR8>(GM));
      const float ex = __builtin_amdgcn_exp2f(gm - GM);
      float GL = asf(gv[2]) * ex;
      GL += dpp<XOR1>(GL); GL += dpp<XOR2>(GL); GL += dpp<HMIR>(GL); GL += dpp<ROR8>(GL);
      const float w = ex / GL, wx = dpp<ROR8>(w);
      const float w0 = (lane & 8) ? wx : w, w1 = (lane & 8) ? w : wx;
      asm volatile("" ::"v"(w0), "v"(w1));
      STAMP(9);
#pragma unroll 1
      for (; spin < SPIN_CAP; ++spin) {
        if (none(((twtags(v0) ^ tag4) | (twtags(v1) ^ tag4)) != 0)) break;
        asm volatile("" ::: "memory");
        v0 = ldsc(rs, ioff, 0);
        v1 = ldsc(rs, ioff + 1024, 0);
      }
      STAMP(10); STAMPV(20, spin + 1);
      float ov[4];
#pragma unroll
      for (int i = 0; i < 4; ++i) ov[i] = twval(v0[i]) * w0 + twval(v1[i]) * w1;
      SB();
#pragma unroll
      for (int i = 0; i < 4; ++i) ov[i] += dpp<XOR1>(ov[i]);
      SB();
#pragma unroll
      for (int i = 0; i < 4; ++i) ov[i] += dpp<XOR2>(ov[i]);
      SB();
#pragma unroll
      for (int i = 0; i < 4; ++i) ov[i] += dpp<HMIR>(ov[i]);
      SB();
      const u32x4 e = {cvtpk(ov[0], ov[0]), cvtpk(ov[1], ov[1]), cvtpk(ov[2], ov[2]), cvtpk(ov[3], ov[3])};
      *(LAS u32x4*)(L + L_OF + (lane >> 3) * 16) = e;
    }
    BARRIER();
    STAMP(11);
    if (rr < NRED) {
      constexpr int NY = RED < 2 ? 4 : 8;
      u32x4 v[NY];
      u32 spin = 0;
#pragma unroll 1
      for (; spin < SPIN_CAP; ++spin) {
        asm volatile("" ::: "memory");
        u32 bad = 0;
#pragma unroll
        for (int i = 0; i < NY; ++i) v[i] = ldsc(rs, yoff, i * (128 / NY) * 2304);
#pragma unroll
        for (int i = 0; i < NY; ++i) bad |= twtags(v[i]) ^ tag4;
        if (none(bad != 0)) break;
      }
      STAMP(12); STAMPV(21, spin + 1);
      float y[4];
#pragma unroll
      for (int k = 0; k < 4; ++k) {
        y[k] = (twval(v[0][k]) + twval(v[1][k])) + (twval(v[2][k]) + twval(v[3][k]));
        if constexpr (NY == 8) y[k] += (twval(v[4 % NY][k]) + twval(v[5 % NY][k])) + (twval(v[6 % NY][k]) + twval(v[7 % NY][k]));
      }
      auto t0 = __builtin_amdgcn_permlane32_swap(__float_as_uint(y[0]), __float_as_uint(y[2]), false, false);
      auto t1 = __builtin_amdgcn_permlane32_swap(__float_as_uint(y[1]), __float_as_uint(y[3]), false, false);
      const float z0 = asf(t0[0]) + asf(t0[1]), z1 = asf(t1[0]) + asf(t1[1]);
      auto t2 = __builtin_amdgcn_permlane16_swap(__float_as_uint(z0), __float_as_uint(z1), false, false);
      float z = asf(t2[0]) + asf(t2[1]);
      z += dpp<ROR8>(z); z += dpp<ROR4>(z);
      if constexpr (RED < 2) z += dpp<XOR2>(z);
      __builtin_amdgcn_raw_buffer_store_b16((unsigned short)cvtpk(z, z), mkrs(out_, 2304), outoff, 0, OAUX);
      if (b == 0) __builtin_amdgcn_raw_buffer_store_b32(tag, rs, lane == 0 ? WS_EPOCH : WS_END, 0, SC1);
    }
    STAMP(13);
    FINISH();
  }
  t0 += (u32)(a * STAG * PACE) / 100u;
  const rsrc_t rv = mkrs((br ? vB : vA) + kvo, kvb);
  u32x4 msk;
#pragma unroll
  for (int k = 0; k < 4; ++k) msk[k] = ((lane & 7) >> 1) == k ? ((lane & 1) ? 0xffff0000u : 0x0000ffffu) : 0u;
  const int colp = (lane & 7) * 8 + ((lane >> 4) & 1) * 4 + (lane >> 5) * 2;
  SB();
  sfor<WIN, 20>([&](auto gc) {
    constexpr int g = decltype(gc)::v;
    pace<g>(t0);
    VMWAIT(WIN - 1);
    if constexpr (g == WIN) STAMP(9);
    ISSUE1(g);
    SB();
    if constexpr (g == (STARTM == 2 ? 6 : 3) + WIN) {
      const u32x4 e = {__builtin_amdgcn_perm(xv[0], xv[0], 0x01000100u), __builtin_amdgcn_perm(xv[0], xv[0], 0x03020302u),
                       __builtin_amdgcn_perm(xv[1], xv[1], 0x01000100u), __builtin_amdgcn_perm(xv[1], xv[1], 0x03020302u)};
      const f32x2 qp = gemv_tile(e, msk, V);
      if (!(lane & 8) && a < 9) *(LAS f32x2*)(L + L_QP + a * 256 + colp * 4) = qp;
      SB();
      STAMP(1);
    }
  });
  const rsrc_t ro = mkrs((rbr ? woB : woA) + (u32)(hc * 256 + rc * 32) * 1152, a < 9 ? 32 * 1152 : 0);
  const u32 wooff = (u32)(4 * (lane >> 3)) * 1152 + (lane & 7) * 16 + a * 128;
#define ISSUE(n) do { if ((n) < 16) V[n] = ldnt(rv, voff, CHUNK(n)); else if ((n) < 20) V[n] = ldnt(ro, wooff + ((n) - 16) * 1152, 0); } while (0)
  float sc[16];
  u32 P[16];
  u32x4 Q;
  u32 tag;
  float m, l = 0.f, F;
  const float LOG2E = 1.4426950408889634f;
  const int yo = (lane & 8) ? WS_END : WS_Y + (rbr * 128 + hc * 8 + rc) * 2304 + (a * 64 + colp) * 4;
  const int ooff = WS_O + (h * 16 + br * 8 + (lane >> 5) * 4 + ((lane & 15) >> 2)) * 2048 + s * 128 + (lane & 3) * 32 + ((lane >> 4) & 1) * 16;
  LAS unsigned char* cntp = L + L_FLAG + 12 - br * 4;
  f32x4 o = {0.f, 0.f, 0.f, 0.f};
  sfor<0, 16 + WIN>([&](auto nc) {
    constexpr int n = decltype(nc)::v;
    u32x4 sel;
    if constexpr (n > QOFF + 6) { sel = msk & P[n - WIN]; asm volatile("" : "+v"(sel)); }
    SB();
    if constexpr (n < 16 + WOPRE) pace<20 + n>(t0);
    VMWAIT(issued_after(n - 1) - (n - WIN + 1));
    sfor<issued_after(n - 1), issued_after(n)>([&](auto kc) { constexpr int k = decltype(kc)::v; ISSUE(k); });
    SB();
    if constexpr (n >= 10 && n < 20) STAMP(n);
    if constexpr (n == QOFF) {
      STAMP(2);
#pragma unroll 1
      for (u32 spin = 0; spin < SPIN_CAP * 8; ++spin) { if (*(volatile LAS u32*)(L + L_FLAG) == 0) break; __builtin_amdgcn_s_sleep(1); }
      asm volatile("" ::: "memory");
      STAMP(3);
      Q = *(LAS u32x4*)(L + L_Q + br * 512 + ((lane >> 3) & 1) * 256 + (lane & 3) * 64 + (lane >> 4) * 16);
      tag = *(LAS u32*)(L + L_FLAG + 4);
      { const int d = __builtin_amdgcn_readfirstlane(*(LAS int*)(L + L_FLAG + 16)); t0 += d > 300 ? 300 : d; }
    }
    if constexpr (n < QOFF) {
    } else if constexpr (n < QOFF + 4) {
      constexpr int T = n - QOFF;
      float d[4];
#pragma unroll
      for (int u = 0; u < 4; ++u) {
        const f32x4 t = mfma(K[T * 4 + u], Q, f32x4{0.f, 0.f, 0.f, 0.f});
        d[u] = ((dpp<0x00>(t[0]) + dpp<0x55>(t[1])) + dpp<0xAA>(t[2])) + dpp<0xFF>(t[3]);
      }
      const u32 r0 = cvtpk(d[0], d[1]), r1 = cvtpk(d[2], d[3]);
      const float SC = 0.08838834764831845f;
      const u32 r2 = cvtpk(bflo(r0) * SC, bfhi(r0) * SC), r3 = cvtpk(bflo(r1) * SC, bfhi(r1) * SC);
      sc[T * 4 + 0] = bflo(r2); sc[T * 4 + 1] = bfhi(r2); sc[T * 4 + 2] = bflo(r3); sc[T * 4 + 3] = bfhi(r3);
    } else if constexpr (n == QOFF + 4) {
      m = sc[0];
#pragma unroll
      for (int i = 1; i < 16; ++i) m = fmaxf(m, sc[i]);
      m = x16_32_max(m);
#pragma unroll
      for (int i = 0; i < 6; ++i) { const float p = __builtin_amdgcn_exp2f((sc[i] - m) * LOG2E); l += p; P[i] = cvtpk(p, p); }
    } else if constexpr (n == QOFF + 5) {
#pragma unroll
      for (int i = 6; i < 16; ++i) { const float p = __builtin_amdgcn_exp2f((sc[i] - m) * LOG2E); l += p; P[i] = cvtpk(p, p); }
      l = x16_32_sum(l);
      if ((lane & 55) == 0) *(LAS f32x2*)(L + L_ML + a * 16 + (lane >> 3) * 8) = f32x2{m * LOG2E, l};
      asm volatile("" ::"v"(yo), "v"(ooff), "v"(cntp));
      STAMP(4);
    } else if constexpr (n == QOFF + 6) {
#pragma unroll
      for (int i = 0; i <= n - WIN; ++i) o = mfma(msk & P[i], V[i], o);
    } else {
      o = mfma(sel, V[n - WIN], o);
      asm volatile("" : "+v"(o));
      if constexpr (n == 15) {
#pragma unroll 1
        for (u32 spin = 0; spin < SPIN_CAP * 8; ++spin) {
          const u32 f = *(volatile LAS u32*)(L + L_F + a * 8 + (lane >> 5) * 4);
          F = __uint_as_float(f);
          if (none(f == SENT)) break;
          __builtin_amdgcn_s_sleep(1);
        }
      }
    }
    SB();
  });
  *(LAS f32x4*)(L + L_O + a * 1024 + lane * 16) = o * F;
  u32 arr;
  asm volatile("s_mov_b64 exec, 1\n\tds_add_rtn_u32 %0, %1, %2\n\ts_mov_b64 exec, -1\n\ts_waitcnt lgkmcnt(0)"
               : "=&v"(arr) : "v"(cntp), "v"(1u) : "memory");
  arr = __builtin_amdgcn_readfirstlane(arr);
  STAMP(5);
  if (arr == (br ? 6u : 0u)) {
    f32x4 t;
    if (br) {
      f32x4 u[8];
#pragma unroll
      for (int k = 0; k < 8; ++k) u[k] = *(LAS f32x4*)(L + L_O + k * 1024 + lane * 16);
      t = ((u[0] + u[1]) + (u[2] + u[3])) + ((u[4] + u[5]) + (u[6] + u[7]));
    } else {
      t = *(LAS f32x4*)(L + L_O + 8 * 1024 + lane * 16) + *(LAS f32x4*)(L + L_O + 9 * 1024 + lane * 16);
    }
    __builtin_amdgcn_raw_buffer_store_b128(u32x4{tw(t[0], tag), tw(t[1], tag), tw(t[2], tag), tw(t[3], tag)}, rs, ooff, 0, SC1);
    STAMP(8);
  }
  if (a == 9) FINISH();
  sfor<16 + WOPRE, 20>([&](auto kc) { constexpr int k = decltype(kc)::v; if constexpr (WOPACE) pace<20 + k>(t0); ISSUE(k); });
  SB();
  BARRIER();
  const u32x4 e = *(LAS u32x4*)(L + L_OF + (lane >> 3) * 16);
  const f32x2 y = gemv_tile(e, msk, V + 16);
  __builtin_amdgcn_raw_buffer_store_b64(u32x2{tw(y[0], tag), tw(y[1], tag)}, rs, yo, 0, SC1);
  STAMP(7);
  FINISH();
}

extern "C" void kernel_launch(void* const* d_in, const int* in_sizes, int n_in, void* d_out, int out_size, void* d_ws,
                              size_t ws_size, hipStream_t stream) {
  typedef const unsigned char* P;
  fused_decode<<<256, NTHR, 0, stream>>>((P)d_in[0], (P)d_in[1], (P)d_in[2], (P)d_in[3], (P)d_in[4], (P)d_in[5], (P)d_in[6],
                                         (P)d_in[7], (P)d_in[8], (P)d_in[9], (unsigned char*)d_out, (unsigned char*)d_ws);
}
```

```cpp
#define STARTM 1
#define QOFF 1
#define WIN 4
#define TSYNC 1
#define STAG 1
#define PACE 342
#define PT0 20
#define CODEB 9000
#define DS 13
#define DW 0
#define DK 0
#define DM 5
#define DV 0
#include <hip/hip_runtime.h>
#include <hip/hip_bf16.h>
#include <stdint.h>

#define STAMPS 0
#define CODEPF 1
#define QSLEEP 16
#define WOM 0
#define MLSLEEP 48
#define WOCNT 10
#define POLLCNT 10
#define PACEN 16
#define WOPRE 1
#define WOPACE 1
#define RED 0
#define OUTPF 0
#define OAUX 16
#define KMAP 1
#define WEND WIN

typedef unsigned int u32;
typedef unsigned long long u64;
typedef u32 u32x2 __attribute__((ext_vector_type(2)));
typedef u32 u32x4 __attribute__((ext_vector_type(4)));
typedef float f32x2 __attribute__((ext_vector_type(2)));
typedef float f32x4 __attribute__((ext_vector_type(4)));
typedef __bf16 bf16x2 __attribute__((ext_vector_type(2)));
typedef __bf16 bf16x8 __attribute__((ext_vector_type(8)));
#define GAS __attribute__((address_space(1)))
#define LAS __attribute__((address_space(3)))
typedef __amdgpu_buffer_rsrc_t rsrc_t;

constexpr int NWAVE = 11, NTHR = NWAVE * 64;
constexpr u32 SENT = 0xFFFFFFFFu;
constexpr u32 SPIN_CAP = 1u << 17;
constexpr int WS_EPOCH = 0;
constexpr int WS_Q = 4096;
constexpr int WS_ML = WS_Q + 256 * 512;
constexpr int WS_O = WS_ML + 256 * 128;
constexpr int WS_Y = WS_O + 256 * 2048;
constexpr int WS_END = WS_Y + 256 * 2304;
constexpr int WS_STAMP = 4 << 20;
constexpr int L_QP = 0;
constexpr int L_ML = 2304;
constexpr int L_F = L_ML + 160;
constexpr int L_FLAG = L_F + 80;
constexpr int L_OF = 2816;
constexpr int L_Q = 3072;
constexpr int L_O = 4096;
constexpr int L_BYTES = L_O + 10 * 1024;
constexpr int SC1 = 16, NT = 2;
constexpr u32 CODE_BYTES = CODEB;

__device__ __forceinline__ rsrc_t mkrs(const void* p, u32 bytes) { return __builtin_amdgcn_make_buffer_rsrc((void*)p, 0, bytes, 0x00020000); }
__device__ __forceinline__ u32x4 ldnt(rsrc_t r, u32 voff, u32 soff) { return __builtin_amdgcn_raw_buffer_load_b128(r, voff, soff, NT); }
__device__ __forceinline__ u32x4 ldsc(rsrc_t r, u32 voff, u32 soff) { return __builtin_amdgcn_raw_buffer_load_b128(r, voff, soff, SC1); }
__device__ __forceinline__ u32 badtag(u32x4 v, u32 tag) { return (v[1] ^ tag) | (v[3] ^ tag); }
__device__ __forceinline__ u32 tw(float x, u32 tag) { return __builtin_amdgcn_perm(__float_as_uint(x), tag, 0x07060500u); }
__device__ __forceinline__ float twval(u32 w) { return __uint_as_float(w & 0xffffff00u); }
__device__ __forceinline__ u32 twtags(u32x4 v) {
  return __builtin_amdgcn_perm(__builtin_amdgcn_perm(v[3], v[2], 0x0c0c0400u), __builtin_amdgcn_perm(v[1], v[0], 0x0c0c0400u), 0x05040100u);
}
__device__ __forceinline__ f32x4 mfma(u32x4 a, u32x4 b, f32x4 c) {
  return __builtin_amdgcn_mfma_f32_16x16x32_bf16(__builtin_bit_cast(bf16x8, a), __builtin_bit_cast(bf16x8, b), c, 0, 0, 0);
}
__device__ __forceinline__ u32 cvtpk(float lo, float hi) {
  bf16x2 r = {(__bf16)lo, (__bf16)hi};
  return __builtin_bit_cast(u32, r);
}
__device__ __forceinline__ float bflo(u32 x) { return __uint_as_float(x << 16); }
__device__ __forceinline__ float bfhi(u32 x) { return __uint_as_float(x & 0xffff0000u); }
__device__ __forceinline__ float asf(u32 x) { return __uint_as_float(x); }
template <int CTRL> __device__ __forceinline__ float dpp(float x) {
  return __builtin_bit_cast(float, __builtin_amdgcn_mov_dpp(__builtin_bit_cast(int, x), CTRL, 0xf, 0xf, true));
}
constexpr int XOR1 = 0xB1, XOR2 = 0x4E, HMIR = 0x141, ROR8 = 0x128, ROR4 = 0x124;
__device__ __forceinline__ float x16_32_max(float x) {
  auto s = __builtin_amdgcn_permlane16_swap(__float_as_uint(x), __float_as_uint(x), false, false);
  x = fmaxf(__uint_as_float(s[0]), __uint_as_float(s[1]));
  auto t = __builtin_amdgcn_permlane32_swap(__float_as_uint(x), __float_as_uint(x), false, false);
  return fmaxf(__uint_as_float(t[0]), __uint_as_float(t[1]));
}
__device__ __forceinline__ float x16_32_sum(float x) {
  auto s = __builtin_amdgcn_permlane16_swap(__float_as_uint(x), __float_as_uint(x), false, false);
  x = __uint_as_float(s[0]) + __uint_as_float(s[1]);
  auto t = __builtin_amdgcn_permlane32_swap(__float_as_uint(x), __float_as_uint(x), false, false);
  return __uint_as_float(t[0]) + __uint_as_float(t[1]);
}
__device__ __forceinline__ bool none(bool p) { return __builtin_amdgcn_ballot_w64(p) == 0; }
__device__ __forceinline__ f32x2 gemv_tile(u32x4 e, u32x4 msk, const u32x4* w) {
  f32x4 y = {0.f, 0.f, 0.f, 0.f};
#pragma unroll
  for (int i = 0; i < 4; ++i) y = mfma(msk & e[i], w[i], y);
  auto t0 = __builtin_amdgcn_permlane32_swap(__float_as_uint(y[0]), __float_as_uint(y[2]), false, false);
  auto t1 = __builtin_amdgcn_permlane32_swap(__float_as_uint(y[1]), __float_as_uint(y[3]), false, false);
  return f32x2{__uint_as_float(t0[0]) + dpp<ROR8>(__uint_as_float(t0[1])), __uint_as_float(t1[0]) + dpp<ROR8>(__uint_as_float(t1[1]))};
}
template <int I> struct IC { static constexpr int v = I; };
template <int LO, int HI, class Fn> __device__ __forceinline__ void sfor(Fn&& f) {
  if constexpr (LO < HI) { f(IC<LO>{}); sfor<LO + 1, HI>(f); }
}
constexpr int issued_after(int n) { return n < 0 ? 0 : (n + 1 < 16 + WOPRE ? n + 1 : 16 + WOPRE); }
#define SB() __builtin_amdgcn_sched_barrier(0)
#define VMWAIT(n) asm volatile("s_waitcnt vmcnt(%0)" ::"n"(n) : "memory")
#define LGKWAIT() asm volatile("s_waitcnt lgkmcnt(0)" ::: "memory")
#define BARRIER() asm volatile("s_waitcnt lgkmcnt(0)\n\ts_barrier" ::: "memory")
#define BAR() asm volatile("s_barrier" ::: "memory")
template <int G> __device__ __forceinline__ void pace(u32 t0) {
  asm volatile("1:\n\ts_memrealtime vcc\n\ts_waitcnt lgkmcnt(0)\n\ts_sub_u32 vcc_lo, vcc_lo, %0\n\ts_cmp_lt_i32 vcc_lo, %1\n\ts_cbranch_scc1 1b"
               : : "s"(t0), "n"(PT0 + G * PACE / 10) : "memory", "scc", "vcc");
}
#define RT(x) asm volatile("s_memrealtime %0\n\ts_waitcnt lgkmcnt(0)" : "=s"(x)::"memory")
#define STAMP(i) do { if ((i) == DS && wave == DW) { if (DV) VMWAIT(0); RT(tdiag); } } while (0)
#define STAMPV(i, v) do {} while (0)
#define FINISH() do { GAS u64* sp_ = (GAS u64*)((GAS unsigned char*)ws_ + (6 << 20)); \
  if (wave == DW && lane == 0) { __hip_atomic_store(sp_ + b, tstart, __ATOMIC_RELAXED, __HIP_MEMORY_SCOPE_AGENT); __hip_atomic_store(sp_ + 256 + b, tdiag, __ATOMIC_RELAXED, __HIP_MEMORY_SCOPE_AGENT); } \
  if (wave == 0 && b == 0) { u64 t; do { __builtin_amdgcn_s_sleep(4); RT(t); } while (t < tstart + 3500); \
    u64 mn = ~0ull, mx = 0, dn = ~0ull, sum = 0; for (int i = 0; i < 4; ++i) { u64 v = __hip_atomic_load(sp_ + i * 64 + lane, __ATOMIC_RELAXED, __HIP_MEMORY_SCOPE_AGENT); u64 d = __hip_atomic_load(sp_ + 256 + i * 64 + lane, __ATOMIC_RELAXED, __HIP_MEMORY_SCOPE_AGENT); mn = v < mn ? v : mn; mx = d > mx ? d : mx; dn = d < dn ? d : dn; sum += d; } \
    for (int m_ = 1; m_ < 64; m_ <<= 1) { u64 o1 = __shfl_xor(mn, m_), o2 = __shfl_xor(mx, m_), o3 = __shfl_xor(dn, m_), o4 = __shfl_xor(sum, m_); mn = o1 < mn ? o1 : mn; mx = o2 > mx ? o2 : mx; dn = o3 < dn ? o3 : dn; sum += o4; } \
    u64 xv_ = DK == 0 ? mx - mn : DK == 1 ? dn - mn : sum / 256 - mn; if (xv_ > 4000) xv_ = 4000; \
    do { __builtin_amdgcn_s_sleep(4); RT(t); } while (t < mn + 5000 + DM * xv_); } __builtin_amdgcn_endpgm(); } while (0)

__global__ void __launch_bounds__(NTHR, 3)
fused_decode(const unsigned char* xA, const unsigned char* kA, const unsigned char* vA, const unsigned char* wqA,
             const unsigned char* woA, const unsigned char* xB, const unsigned char* kB, const unsigned char* vB,
             const unsigned char* wqB, const unsigned char* woB, unsigned char* out_, unsigned char* ws_) {
  __shared__ __attribute__((aligned(16))) unsigned char smem[L_BYTES];
  LAS unsigned char* L = (LAS unsigned char*)smem;
  u64 pc;
  asm volatile("s_getpc_b64 %0" : "=s"(pc));
  const int b = blockIdx.x, lane = threadIdx.x & 63;
  const int wave = __builtin_amdgcn_readfirstlane(threadIdx.x >> 6);
  const int x = b & 7, j = b >> 3;
#define HEAD_OF(x_, k_) ((((x_) & 1) ^ 1) * 8 + ((x_) >> 1) * 2 + (k_))
  const int h = HEAD_OF(x, j >> 4), s = j & 15;
  const int rbr = x >> 2, rh = (x >> 1) & 1, cb = (x & 1) * 32 + j;
  const int xs = x ^ (1 + 3 * (j >> 4));
  const int hc = HEAD_OF(xs, (j >> 3) & 1), rc = j & 7;
  const rsrc_t rs = mkrs(ws_, WS_END);
  u64 tstart, tdiag; RT(tstart); tdiag = tstart;
  u32 t0 = PACE ? (u32)__builtin_amdgcn_s_memrealtime() : 0u;
  *(LAS u32*)(L + threadIdx.x * 4) = SENT;
  BARRIER();
  asm volatile("" ::"s"(xA), "s"(kA), "s"(vA), "s"(wqA), "s"(woA), "s"(xB), "s"(kB), "s"(vB), "s"(wqB), "s"(woB), "s"(out_), "s"(ws_));
  const int a = wave - 1, br = a < 8;
  u32x4 K[16], V[20];
  const int row0 = rh * 288 + a * 32;
  const rsrc_t rq = mkrs(rbr ? wqB : wqA, (u32)a < 9u ? 576 * 8192 : 0);
  const u32 qoff = (u32)(row0 + 4 * (lane >> 3)) * 8192 + cb * 128 + (lane & 7) * 16;
  const u32x2 xv = __builtin_amdgcn_raw_buffer_load_b64(mkrs(rbr ? xB : xA, (u32)a < 9u ? 1152 : 0), (row0 + 4 * (lane >> 3)) * 2, 0, 0);
  const u32 kvo = (u32)(h * 2048 + (br ? 0 : 1536 - 128) + a * 16 + s) * 1024;
  const u32 sh = br ? 17 : 15, kvb = wave == 0 ? 0 : ((15u << sh) + 1024), cx = h;
  const rsrc_t rk = mkrs((br ? kB : kA) + kvo, kvb);
  const u32 koff = (u32)((lane >> 2) & 3) * 256 + (lane & 3) * 64 + (lane >> 4) * 16, voff = lane * 16;
#define CHUNK(j_) ((u32)((j_) ^ cx) << sh)
#define P1_ISWQ(g) (STARTM == 2 ? ((g) < 8 && !((g) & 1)) : (g) < 4)
#define P1_IDX(g) (STARTM == 2 ? ((g) < 8 ? (g) >> 1 : (g) - 4) : ((g) < 4 ? (g) : (g) - 4))
#define ISSUE1(g) do { if constexpr (P1_ISWQ(g)) V[P1_IDX(g)] = ldnt(rq, qoff, P1_IDX(g) * 8192); else K[P1_IDX(g)] = ldnt(rk, koff, CHUNK(P1_IDX(g))); } while (0)
  sfor<0, WIN>([&](auto gc) { constexpr int g = decltype(gc)::v; if constexpr (g > 0) BAR(); ISSUE1(g); });
  SB();
  if (wave == 0) {
    const u32 tag = __builtin_amdgcn_raw_buffer_load_b32(rs, WS_EPOCH, 0, SC1) + 1u;
    {
      const rsrc_t rcode = mkrs((const void*)(pc & ~(u64)255), CODE_BYTES);
      const u32 c0 = __builtin_amdgcn_raw_buffer_load_b32(rcode, lane * 128, 0, 0);
      const u32 c1 = __builtin_amdgcn_raw_buffer_load_b32(rcode, lane * 128, 8192, 0);
      asm volatile("" ::"v"(c0), "v"(c1));
    }
    *(LAS u32*)(L + L_FLAG + 4) = tag;
    const u32 tag4 = __builtin_amdgcn_perm(tag, tag, 0u);
    STAMP(1);
    {
      u32 t[9];
#pragma unroll 1
      for (u32 spin = 0; spin < SPIN_CAP * 8; ++spin) {
#pragma unroll
        for (int k = 0; k < 9; ++k) t[k] = *(volatile LAS u32*)(L + L_QP + k * 256 + lane * 4);
        if (none(max(max(max(max(t[0], t[1]), max(t[2], t[3])), max(max(t[4], t[5]), max(t[6], t[7]))), t[8]) == SENT)) break;
        __builtin_amdgcn_s_sleep(1);
      }
      const float qp = ((asf(t[0]) + asf(t[1])) + (asf(t[2]) + asf(t[3]))) + ((asf(t[4]) + asf(t[5])) + (asf(t[6]) + asf(t[7]))) + asf(t[8]);
      __builtin_amdgcn_raw_buffer_store_b64(u32x2{__float_as_uint(qp), TSYNC ? (tag & 0xffffu) | (t0 << 16) : tag}, rs, WS_Q + ((rbr * 2 + rh) * 64 + cb) * 512 + lane * 8, 0, SC1);
    }
    STAMP(2);
    {
      u32x4 v[8];
      u32 spin = 0;
#pragma unroll 1
      for (; spin < SPIN_CAP; ++spin) {
        __builtin_amdgcn_s_sleep(QSLEEP);
        asm volatile("" ::: "memory");
        u32 bad = 0;
#pragma unroll
        for (int i = 0; i < 8; ++i) v[i] = ldsc(rs, WS_Q + h * 2048 + lane * 16 + ((i >> 1) & 1) * 1024, ((i >> 2) * 2 + (i & 1)) * 32768);
#pragma unroll
        for (int i = 0; i < 8; ++i) bad |= badtag(v[i], tag);
        if (TSYNC) bad &= 0xffffu;
        if (none(bad != 0)) break;
      }
      STAMPV(22, spin + 1);
      {
        const int d = (((int)(v[0][1] - (t0 << 16)) >> 16) + ((int)(v[1][1] - (t0 << 16)) >> 16) + ((int)(v[4][1] - (t0 << 16)) >> 16) + ((int)(v[5][1] - (t0 << 16)) >> 16)) >> 2;
        *(LAS int*)(L + L_FLAG + 16) = __builtin_amdgcn_readfirstlane(d);
      }
#pragma unroll
      for (int i = 0; i < 4; ++i)
        *(LAS u32*)(L + L_Q + i * 256 + lane * 4) = cvtpk(asf(v[2 * i][0]) + asf(v[2 * i + 1][0]), asf(v[2 * i][2]) + asf(v[2 * i + 1][2]));
      LGKWAIT();
      *(volatile LAS u32*)(L + L_FLAG) = 0;
    }
    STAMP(4);
    {
      u32x2 ml;
#pragma unroll 1
      for (u32 spin = 0; spin < SPIN_CAP * 8; ++spin) {
        ml = *(volatile LAS u32x2*)(L + L_ML + (lane < 20 ? lane : 0) * 8);
        if (none(max(ml[0], ml[1]) == SENT)) break;
        __builtin_amdgcn_s_sleep(2);
      }
      STAMP(6);
      const bool live = lane < 20;
      float m = live ? asf(ml[0]) : -INFINITY;
      float M = fmaxf(m, dpp<XOR2>(m)); M = fmaxf(M, dpp<ROR4>(M)); M = fmaxf(M, dpp<ROR8>(M));
      const float F = __builtin_amdgcn_exp2f(m - M);
      if (live) *(LAS float*)(L + L_F + lane * 4) = F;
      float l = live ? asf(ml[1]) * F : 0.f;
      l += dpp<XOR2>(l); l += dpp<ROR4>(l); l += dpp<ROR8>(l);
      __builtin_amdgcn_raw_buffer_store_b128(u32x4{__float_as_uint(M), tag, __float_as_uint(l), tag}, rs,
                                             (lane & 46) == 0 ? WS_ML + (h * 16 + s) * 128 + ((lane < 16 ? 2 : 0) + (lane & 1)) * 16 : WS_END, 0, SC1);
    }
    const int moff = WS_ML + (hc * 16 + (lane & 15)) * 128 + (rbr * 2 + (rc >> 2)) * 16;
    const int ioff = WS_O + (hc * 16 + rbr * 8 + rc) * 2048 + (lane & 7) * 128 + (lane >> 3) * 16;
    const int br2 = rbr ^ 1;
    const int rr = ((j >> 2) * 4 + (x & 3)) * 4 + (j & 3);
    constexpr int NRED = 72;
    const int yoff = WS_Y + (br2 * 128 + (lane >> 1)) * 2304 + rr * 32 + (lane & 1) * 16;
    const int outoff = (lane & 14) == 0 ? (br2 * 576 + rr * 8 + 4 * (lane & 1) + (lane >> 4)) * 2 : 4096;
    asm volatile("" ::"v"(yoff), "v"(ioff), "v"(moff), "v"(outoff));
    STAMP(7);
#pragma unroll 1
    for (u32 spin = 0; spin < SPIN_CAP * 8; ++spin) {
      const u32x2 c = *(volatile LAS u32x2*)(L + L_FLAG + 8);
      if ((int)(c[0] + c[1]) >= POLLCNT - 2) break;
      __builtin_amdgcn_s_sleep(1);
    }
    STAMP(8);
    {
      u32x4 v0, v1, gv;
      u32 spin = 0;
#pragma unroll 1
      for (; spin < SPIN_CAP; ++spin) {
        asm volatile("" ::: "memory");
        gv = ldsc(rs, moff, 0);
        v0 = ldsc(rs, ioff, 0);
        v1 = ldsc(rs, ioff + 1024, 0);
        if (none(badtag(gv, tag) != 0)) break;
      }
      const float gm = asf(gv[0]);
      float GM = fmaxf(gm, dpp<XOR1>(gm)); GM = fmaxf(GM, dpp<XOR2>(GM)); GM = fmaxf(GM, dpp<HMIR>(GM)); GM = fmaxf(GM, dpp<ROR8>(GM));
      const float ex = __builtin_amdgcn_exp2f(gm - GM);
      float GL = asf(gv[2]) * ex;
      GL += dpp<XOR1>(GL); GL += dpp<XOR2>(GL); GL += dpp<HMIR>(GL); GL += dpp<ROR8>(GL);
      const float w = ex / GL, wx = dpp<ROR8>(w);
      const float w0 = (lane & 8) ? wx : w, w1 = (lane & 8) ? w : wx;
      asm volatile("" ::"v"(w0), "v"(w1));
      STAMP(9);
#pragma unroll 1
      for (; spin < SPIN_CAP; ++spin) {
        if (none(((twtags(v0) ^ tag4) | (twtags(v1) ^ tag4)) != 0)) break;
        asm volatile("" ::: "memory");
        v0 = ldsc(rs, ioff, 0);
        v1 = ldsc(rs, ioff + 1024, 0);
      }
      STAMP(10); STAMPV(20, spin + 1);
      float ov[4];
#pragma unroll
      for (int i = 0; i < 4; ++i) ov[i] = twval(v0[i]) * w0 + twval(v1[i]) * w1;
      SB();
#pragma unroll
      for (int i = 0; i < 4; ++i) ov[i] += dpp<XOR1>(ov[i]);
      SB();
#pragma unroll
      for (int i = 0; i < 4; ++i) ov[i] += dpp<XOR2>(ov[i]);
      SB();
#pragma unroll
      for (int i = 0; i < 4; ++i) ov[i] += dpp<HMIR>(ov[i]);
      SB();
      const u32x4 e = {cvtpk(ov[0], ov[0]), cvtpk(ov[1], ov[1]), cvtpk(ov[2], ov[2]), cvtpk(ov[3], ov[3])};
      *(LAS u32x4*)(L + L_OF + (lane >> 3) * 16) = e;
    }
    BARRIER();
    STAMP(11);
    if (rr < NRED) {
      constexpr int NY = RED < 2 ? 4 : 8;
      u32x4 v[NY];
      u32 spin = 0;
#pragma unroll 1
      for (; spin < SPIN_CAP; ++spin) {
        asm volatile("" ::: "memory");
        u32 bad = 0;
#pragma unroll
        for (int i = 0; i < NY; ++i) v[i] = ldsc(rs, yoff, i * (128 / NY) * 2304);
#pragma unroll
        for (int i = 0; i < NY; ++i) bad |= twtags(v[i]) ^ tag4;
        if (none(bad != 0)) break;
      }
      STAMP(12); STAMPV(21, spin + 1);
      float y[4];
#pragma unroll
      for (int k = 0; k < 4; ++k) {
        y[k] = (twval(v[0][k]) + twval(v[1][k])) + (twval(v[2][k]) + twval(v[3][k]));
        if constexpr (NY == 8) y[k] += (twval(v[4 % NY][k]) + twval(v[5 % NY][k])) + (twval(v[6 % NY][k]) + twval(v[7 % NY][k]));
      }
      auto t0 = __builtin_amdgcn_permlane32_swap(__float_as_uint(y[0]), __float_as_uint(y[2]), false, false);
      auto t1 = __builtin_amdgcn_permlane32_swap(__float_as_uint(y[1]), __float_as_uint(y[3]), false, false);
      const float z0 = asf(t0[0]) + asf(t0[1]), z1 = asf(t1[0]) + asf(t1[1]);
      auto t2 = __builtin_amdgcn_permlane16_swap(__float_as_uint(z0), __float_as_uint(z1), false, false);
      float z = asf(t2[0]) + asf(t2[1]);
      z += dpp<ROR8>(z); z += dpp<ROR4>(z);
      if constexpr (RED < 2) z += dpp<XOR2>(z);
      __builtin_amdgcn_raw_buffer_store_b16((unsigned short)cvtpk(z, z), mkrs(out_, 2304), outoff, 0, OAUX);
      if (b == 0) __builtin_amdgcn_raw_buffer_store_b32(tag, rs, lane == 0 ? WS_EPOCH : WS_END, 0, SC1);
    }
    STAMP(13);
    FINISH();
  }
  t0 += (u32)(a * STAG * PACE) / 100u;
  const rsrc_t rv = mkrs((br ? vB : vA) + kvo, kvb);
  u32x4 msk;
#pragma unroll
  for (int k = 0; k < 4; ++k) msk[k] = ((lane & 7) >> 1) == k ? ((lane & 1) ? 0xffff0000u : 0x0000ffffu) : 0u;
  const int colp = (lane & 7) * 8 + ((lane >> 4) & 1) * 4 + (lane >> 5) * 2;
  SB();
  sfor<WIN, 20>([&](auto gc) {
    constexpr int g = decltype(gc)::v;
    pace<g>(t0);
    VMWAIT(WIN - 1);
    if constexpr (g == WIN) STAMP(9);
    ISSUE1(g);
    SB();
    if constexpr (g == (STARTM == 2 ? 6 : 3) + WIN) {
      const u32x4 e = {__builtin_amdgcn_perm(xv[0], xv[0], 0x01000100u), __builtin_amdgcn_perm(xv[0], xv[0], 0x03020302u),
                       __builtin_amdgcn_perm(xv[1], xv[1], 0x01000100u), __builtin_amdgcn_perm(xv[1], xv[1], 0x03020302u)};
      const f32x2 qp = gemv_tile(e, msk, V);
      if (!(lane & 8) && a < 9) *(LAS f32x2*)(L + L_QP + a * 256 + colp * 4) = qp;
      SB();
      STAMP(1);
    }
  });
  const rsrc_t ro = mkrs((rbr ? woB : woA) + (u32)(hc * 256 + rc * 32) * 1152, a < 9 ? 32 * 1152 : 0);
  const u32 wooff = (u32)(4 * (lane >> 3)) * 1152 + (lane & 7) * 16 + a * 128;
#define ISSUE(n) do { if ((n) < 16) V[n] = ldnt(rv, voff, CHUNK(n)); else if ((n) < 20) V[n] = ldnt(ro, wooff + ((n) - 16) * 1152, 0); } while (0)
  float sc[16];
  u32 P[16];
  u32x4 Q;
  u32 tag;
  float m, l = 0.f, F;
  const float LOG2E = 1.4426950408889634f;
  const int yo = (lane & 8) ? WS_END : WS_Y + (rbr * 128 + hc * 8 + rc) * 2304 + (a * 64 + colp) * 4;
  const int ooff = WS_O + (h * 16 + br * 8 + (lane >> 5) * 4 + ((lane & 15) >> 2)) * 2048 + s * 128 + (lane & 3) * 32 + ((lane >> 4) & 1) * 16;
  LAS unsigned char* cntp = L + L_FLAG + 12 - br * 4;
  f32x4 o = {0.f, 0.f, 0.f, 0.f};
  sfor<0, 16 + WIN>([&](auto nc) {
    constexpr int n = decltype(nc)::v;
    u32x4 sel;
    if constexpr (n > QOFF + 6) { sel = msk & P[n - WIN]; asm volatile("" : "+v"(sel)); }
    SB();
    if constexpr (n < 16 + WOPRE) pace<20 + n>(t0);
    VMWAIT(issued_after(n - 1) - (n - WIN + 1));
    sfor<issued_after(n - 1), issued_after(n)>([&](auto kc) { constexpr int k = decltype(kc)::v; ISSUE(k); });
    SB();
    if constexpr (n == QOFF) {
      STAMP(2);
#pragma unroll 1
      for (u32 spin = 0; spin < SPIN_CAP * 8; ++spin) { if (*(volatile LAS u32*)(L + L_FLAG) == 0) break; __builtin_amdgcn_s_sleep(1); }
      asm volatile("" ::: "memory");
      STAMP(3);
      Q = *(LAS u32x4*)(L + L_Q + br * 512 + ((lane >> 3) & 1) * 256 + (lane & 3) * 64 + (lane >> 4) * 16);
      tag = *(LAS u32*)(L + L_FLAG + 4);
      { const int d = __builtin_amdgcn_readfirstlane(*(LAS int*)(L + L_FLAG + 16)); t0 += d > 300 ? 300 : d; }
    }
    if constexpr (n < QOFF) {
    } else if constexpr (n < QOFF + 4) {
      constexpr int T = n - QOFF;
      float d[4];
#pragma unroll
      for (int u = 0; u < 4; ++u) {
        const f32x4 t = mfma(K[T * 4 + u], Q, f32x4{0.f, 0.f, 0.f, 0.f});
        d[u] = ((dpp<0x00>(t[0]) + dpp<0x55>(t[1])) + dpp<0xAA>(t[2])) + dpp<0xFF>(t[3]);
      }
      const u32 r0 = cvtpk(d[0], d[1]), r1 = cvtpk(d[2], d[3]);
      const float SC = 0.08838834764831845f;
      const u32 r2 = cvtpk(bflo(r0) * SC, bfhi(r0) * SC), r3 = cvtpk(bflo(r1) * SC, bfhi(r1) * SC);
      sc[T * 4 + 0] = bflo(r2); sc[T * 4 + 1] = bfhi(r2); sc[T * 4 + 2] = bflo(r3); sc[T * 4 + 3] = bfhi(r3);
    } else if constexpr (n == QOFF + 4) {
      m = sc[0];
#pragma unroll
      for (int i = 1; i < 16; ++i) m = fmaxf(m, sc[i]);
      m = x16_32_max(m);
#pragma unroll
      for (int i = 0; i < 6; ++i) { const float p = __builtin_amdgcn_exp2f((sc[i] - m) * LOG2E); l += p; P[i] = cvtpk(p, p); }
    } else if constexpr (n == QOFF + 5) {
#pragma unroll
      for (int i = 6; i < 16; ++i) { const float p = __builtin_amdgcn_exp2f((sc[i] - m) * LOG2E); l += p; P[i] = cvtpk(p, p); }
      l = x16_32_sum(l);
      if ((lane & 55) == 0) *(LAS f32x2*)(L + L_ML + a * 16 + (lane >> 3) * 8) = f32x2{m * LOG2E, l};
      asm volatile("" ::"v"(yo), "v"(ooff), "v"(cntp));
      STAMP(4);
    } else if constexpr (n == QOFF + 6) {
#pragma unroll
      for (int i = 0; i <= n - WIN; ++i) o = mfma(msk & P[i], V[i], o);
    } else {
      o = mfma(sel, V[n - WIN], o);
      asm volatile("" : "+v"(o));
      if constexpr (n == 15) {
#pragma unroll 1
        for (u32 spin = 0; spin < SPIN_CAP * 8; ++spin) {
          const u32 f = *(volatile LAS u32*)(L + L_F + a * 8 + (lane >> 5) * 4);
          F = __uint_as_float(f);
          if (none(f == SENT)) break;
          __builtin_amdgcn_s_sleep(1);
        }
      }
    }
    SB();
  });
  *(LAS f32x4*)(L + L_O + a * 1024 + lane * 16) = o * F;
  u32 arr;
  asm volatile("s_mov_b64 exec, 1\n\tds_add_rtn_u32 %0, %1, %2\n\ts_mov_b64 exec, -1\n\ts_waitcnt lgkmcnt(0)"
               : "=&v"(arr) : "v"(cntp), "v"(1u) : "memory");
  arr = __builtin_amdgcn_readfirstlane(arr);
  STAMP(5);
  if (arr == (br ? 6u : 0u)) {
    f32x4 t;
    if (br) {
      f32x4 u[8];
#pragma unroll
      for (int k = 0; k < 8; ++k) u[k] = *(LAS f32x4*)(L + L_O + k * 1024 + lane * 16);
      t = ((u[0] + u[1]) + (u[2] + u[3])) + ((u[4] + u[5]) + (u[6] + u[7]));
    } else {
      t = *(LAS f32x4*)(L + L_O + 8 * 1024 + lane * 16) + *(LAS f32x4*)(L + L_O + 9 * 1024 + lane * 16);
    }
    __builtin_amdgcn_raw_buffer_store_b128(u32x4{tw(t[0], tag), tw(t[1], tag), tw(t[2], tag), tw(t[3], tag)}, rs, ooff, 0, SC1);
    STAMP(8);
  }
  if (a == 9) FINISH();
  sfor<16 + WOPRE, 20>([&](auto kc) { constexpr int k = decltype(kc)::v; if constexpr (WOPACE) pace<20 + k>(t0); ISSUE(k); });
  SB();
  BARRIER();
  const u32x4 e = *(LAS u32x4*)(L + L_OF + (lane >> 3) * 16);
  const f32x2 y = gemv_tile(e, msk, V + 16);
  __builtin_amdgcn_raw_buffer_store_b64(u32x2{tw(y[0], tag), tw(y[1], tag)}, rs, yo, 0, SC1);
  STAMP(7);
  FINISH();
}

extern "C" void kernel_launch(void* const* d_in, const int* in_sizes, int n_in, void* d_out, int out_size, void* d_ws,
                              size_t ws_size, hipStream_t stream) {
  typedef const unsigned char* P;
  fused_decode<<<256, NTHR, 0, stream>>>((P)d_in[0], (P)d_in[1], (P)d_in[2], (P)d_in[3], (P)d_in[4], (P)d_in[5], (P)d_in[6],
                                         (P)d_in[7], (P)d_in[8], (P)d_in[9], (unsigned char*)d_out, (unsigned char*)d_ws);
}
```

```cpp
#define PACE 336
#define PT0 150
#define TOUCH 0
#define D1 1250
#define D2 16
#define CODEB 8800
#define DS 13
#define DW 0
#define DK 0
#define DM 5
#define DV 0
#include <hip/hip_runtime.h>
#include <hip/hip_bf16.h>
#include <stdint.h>

#define STAMPS 0
#define WIN 4
#define QSLEEP 16
#define QOFF 1
#define PWO PACE
#define WOPRE 1
#define STAG 0
#define MIX 1

typedef unsigned int u32;
typedef unsigned long long u64;
typedef u32 u32x2 __attribute__((ext_vector_type(2)));
typedef u32 u32x4 __attribute__((ext_vector_type(4)));
typedef float f32x2 __attribute__((ext_vector_type(2)));
typedef float f32x4 __attribute__((ext_vector_type(4)));
typedef __bf16 bf16x2 __attribute__((ext_vector_type(2)));
typedef __bf16 bf16x8 __attribute__((ext_vector_type(8)));
#define GAS __attribute__((address_space(1)))
#define LAS __attribute__((address_space(3)))
typedef __amdgpu_buffer_rsrc_t rsrc_t;

constexpr int NWAVE = 11, NTHR = NWAVE * 64;
constexpr u32 SENT = 0xFFFFFFFFu;
constexpr u32 SPIN_CAP = 1u << 17;
constexpr int WS_EPOCH = 0;
constexpr int WS_Q = 4096;
constexpr int WS_ML = WS_Q + 256 * 512;
constexpr int WS_O = WS_ML + 256 * 128;
constexpr int WS_Y = WS_O + 256 * 2048;
constexpr int WS_END = WS_Y + 256 * 2304;
constexpr int WS_STAMP = 4 << 20;
constexpr int L_QP = 0;
constexpr int L_ML = 2304;
constexpr int L_F = L_ML + 160;
constexpr int L_FLAG = L_F + 80;
constexpr int L_T0 = 2816;
constexpr int L_OF = 2944;
constexpr int L_Q = 3072;
constexpr int L_O = 4096;
constexpr int L_BYTES = L_O + 10 * 1024;
constexpr int SC1 = 16, NT = 2;
constexpr u32 CODE_BYTES = CODEB;

__device__ __forceinline__ rsrc_t mkrs(const void* p, u32 bytes) { return __builtin_amdgcn_make_buffer_rsrc((void*)p, 0, bytes, 0x00020000); }
__device__ __forceinline__ u32x4 ldnt(rsrc_t r, u32 voff, u32 soff) { return __builtin_amdgcn_raw_buffer_load_b128(r, voff, soff, NT); }
__device__ __forceinline__ u32x4 ldsc(rsrc_t r, u32 voff, u32 soff) { return __builtin_amdgcn_raw_buffer_load_b128(r, voff, soff, SC1); }
__device__ __forceinline__ u32 badtag(u32x4 v, u32 tag) { return (v[1] ^ tag) | (v[3] ^ tag); }
__device__ __forceinline__ u32 tw(float x, u32 tag) { return __builtin_amdgcn_perm(__float_as_uint(x), tag, 0x07060500u); }
__device__ __forceinline__ u32 or4(u32x4 v) { return (v[0] | v[1]) | (v[2] | v[3]); }
__device__ __forceinline__ f32x4 mfma(u32x4 a, u32x4 b, f32x4 c) {
  return __builtin_amdgcn_mfma_f32_16x16x32_bf16(__builtin_bit_cast(bf16x8, a), __builtin_bit_cast(bf16x8, b), c, 0, 0, 0);
}
__device__ __forceinline__ u32 cvtpk(float lo, float hi) {
  bf16x2 r = {(__bf16)lo, (__bf16)hi};
  return __builtin_bit_cast(u32, r);
}
__device__ __forceinline__ float bflo(u32 x) { return __uint_as_float(x << 16); }
__device__ __forceinline__ float bfhi(u32 x) { return __uint_as_float(x & 0xffff0000u); }
__device__ __forceinline__ float asf(u32 x) { return __uint_as_float(x); }
__device__ __forceinline__ f32x4 asf4(u32x4 x) { return __builtin_bit_cast(f32x4, x); }
template <int CTRL> __device__ __forceinline__ float dpp(float x) {
  return __builtin_bit_cast(float, __builtin_amdgcn_mov_dpp(__builtin_bit_cast(int, x), CTRL, 0xf, 0xf, true));
}
template <int CTRL> __device__ __forceinline__ int dppi(int x) { return __builtin_amdgcn_mov_dpp(x, CTRL, 0xf, 0xf, true); }
constexpr int XOR1 = 0xB1, XOR2 = 0x4E, HMIR = 0x141, ROR8 = 0x128, ROR4 = 0x124;
__device__ __forceinline__ float x16_32_max(float x) {
  auto s = __builtin_amdgcn_permlane16_swap(__float_as_uint(x), __float_as_uint(x), false, false);
  x = fmaxf(__uint_as_float(s[0]), __uint_as_float(s[1]));
  auto t = __builtin_amdgcn_permlane32_swap(__float_as_uint(x), __float_as_uint(x), false, false);
  return fmaxf(__uint_as_float(t[0]), __uint_as_float(t[1]));
}
__device__ __forceinline__ float x16_32_sum(float x) {
  auto s = __builtin_amdgcn_permlane16_swap(__float_as_uint(x), __float_as_uint(x), false, false);
  x = __uint_as_float(s[0]) + __uint_as_float(s[1]);
  auto t = __builtin_amdgcn_permlane32_swap(__float_as_uint(x), __float_as_uint(x), false, false);
  return __uint_as_float(t[0]) + __uint_as_float(t[1]);
}
__device__ __forceinline__ bool none(bool p) { return __builtin_amdgcn_ballot_w64(p) == 0; }
__device__ __forceinline__ f32x2 gemv_tile(u32x4 e, u32x4 msk, const u32x4* w) {
  f32x4 y = {0.f, 0.f, 0.f, 0.f};
#pragma unroll
  for (int i = 0; i < 4; ++i) y = mfma(msk & e[i], w[i], y);
  auto t0 = __builtin_amdgcn_permlane32_swap(__float_as_uint(y[0]), __float_as_uint(y[2]), false, false);
  auto t1 = __builtin_amdgcn_permlane32_swap(__float_as_uint(y[1]), __float_as_uint(y[3]), false, false);
  return f32x2{__uint_as_float(t0[0]) + dpp<ROR8>(__uint_as_float(t0[1])), __uint_as_float(t1[0]) + dpp<ROR8>(__uint_as_float(t1[1]))};
}
template <int I> struct IC { static constexpr int v = I; };
template <int LO, int HI, class Fn> __device__ __forceinline__ void sfor(Fn&& f) {
  if constexpr (LO < HI) { f(IC<LO>{}); sfor<LO + 1, HI>(f); }
}
constexpr int issued_after(int n) { return n < 0 ? 0 : (n + 1 < 16 + WOPRE ? n + 1 : 16 + WOPRE); }
constexpr int slot_ns(int g) { return PT0 + (g <= 36 ? g * PACE : 36 * PACE + (g - 36) * PWO); }
#define SB() __builtin_amdgcn_sched_barrier(0)
#define VMWAIT(n) asm volatile("s_waitcnt vmcnt(%0)" ::"n"(n) : "memory")
#define LGKWAIT() asm volatile("s_waitcnt lgkmcnt(0)" ::: "memory")
#define BARRIER() asm volatile("s_waitcnt lgkmcnt(0)\n\ts_barrier" ::: "memory")
template <int NS> __device__ __forceinline__ void until(u32 t0) {
  asm volatile("1:\n\ts_memrealtime vcc\n\ts_waitcnt lgkmcnt(0)\n\ts_sub_u32 vcc_lo, vcc_lo, %0\n\ts_cmp_lt_i32 vcc_lo, %1\n\ts_cbranch_scc1 1b"
               : : "s"(t0), "n"(NS / 10) : "memory", "scc", "vcc");
}
#define RT(x) asm volatile("s_memrealtime %0\n\ts_waitcnt lgkmcnt(0)" : "=s"(x)::"memory")
#define STAMP(i) do { if ((i) == DS && wave == DW) { if (DV) VMWAIT(0); RT(tdiag); } } while (0)
#define STAMPV(i, v) do {} while (0)
#define FINISH() do { GAS u64* sp_ = (GAS u64*)((GAS unsigned char*)ws_ + (6 << 20)); \
  if (wave == DW && lane == 0) { __hip_atomic_store(sp_ + b, tstart, __ATOMIC_RELAXED, __HIP_MEMORY_SCOPE_AGENT); __hip_atomic_store(sp_ + 256 + b, tdiag, __ATOMIC_RELAXED, __HIP_MEMORY_SCOPE_AGENT); } \
  if (wave == 0 && b == 0) { u64 t; do { __builtin_amdgcn_s_sleep(4); RT(t); } while (t < tstart + 3500); \
    u64 mn = ~0ull, mx = 0, dn = ~0ull, sum = 0; for (int i = 0; i < 4; ++i) { u64 v = __hip_atomic_load(sp_ + i * 64 + lane, __ATOMIC_RELAXED, __HIP_MEMORY_SCOPE_AGENT); u64 d = __hip_atomic_load(sp_ + 256 + i * 64 + lane, __ATOMIC_RELAXED, __HIP_MEMORY_SCOPE_AGENT); mn = v < mn ? v : mn; mx = d > mx ? d : mx; dn = d < dn ? d : dn; sum += d; } \
    for (int m_ = 1; m_ < 64; m_ <<= 1) { u64 o1 = __shfl_xor(mn, m_), o2 = __shfl_xor(mx, m_), o3 = __shfl_xor(dn, m_), o4 = __shfl_xor(sum, m_); mn = o1 < mn ? o1 : mn; mx = o2 > mx ? o2 : mx; dn = o3 < dn ? o3 : dn; sum += o4; } \
    u64 xv_ = DK == 0 ? mx - mn : DK == 1 ? dn - mn : sum / 256 - mn; if (xv_ > 4000) xv_ = 4000; \
    do { __builtin_amdgcn_s_sleep(4); RT(t); } while (t < mn + 5000 + DM * xv_); } __builtin_amdgcn_endpgm(); } while (0)

__global__ void __launch_bounds__(NTHR, 3)
fused_decode(const unsigned char* xA, const unsigned char* kA, const unsigned char* vA, const unsigned char* wqA,
             const unsigned char* woA, const unsigned char* xB, const unsigned char* kB, const unsigned char* vB,
             const unsigned char* wqB, const unsigned char* woB, unsigned char* out_, unsigned char* ws_) {
  __shared__ __attribute__((aligned(16))) unsigned char smem[L_BYTES];
  LAS unsigned char* L = (LAS unsigned char*)smem;
  u64 tstart, tdiag;
  asm volatile("s_memrealtime %0" : "=s"(tstart));
  const int b = blockIdx.x, lane = threadIdx.x & 63;
  const int wave = __builtin_amdgcn_readfirstlane(threadIdx.x >> 6);
  const int x = b & 7, j = b >> 3;
  const int a = wave - 1, br = a < 8;
  const int rbr = x >> 2, rh = (x >> 1) & 1, cb = (x & 1) * 32 + j;
  const int row = rh * 288 + a * 32 + 4 * (lane >> 3);
  const u32 qoff = (u32)row * 8192 + cb * 128 + (lane & 7) * 16;
  const u32 nq = (u32)a < 9u ? 576 * 8192 : 0, nx = (u32)a < 9u ? 1152 : 0;
  asm volatile("" ::"v"(qoff), "v"(row * 2), "s"(nq), "s"(nx));
  SB();
  asm volatile("" ::"s"(xA), "s"(kA), "s"(vA), "s"(wqA), "s"(woA), "s"(xB), "s"(kB), "s"(vB), "s"(wqB), "s"(woB), "s"(out_), "s"(ws_));
  u32x4 K[16], V[20];
  const rsrc_t rq = mkrs(rbr ? wqB : wqA, nq);
#pragma unroll
  for (int i = 0; i < 4; ++i) V[i] = ldnt(rq, qoff, i * 8192);
  const u32x2 xv = __builtin_amdgcn_raw_buffer_load_b64(mkrs(rbr ? xB : xA, nx), row * 2, 0, 0);
  SB();
  u32 t0 = (u32)__builtin_amdgcn_s_memrealtime();
  u64 pc;
  asm volatile("s_getpc_b64 %0" : "=s"(pc));
  asm volatile("s_waitcnt lgkmcnt(0)" : "+s"(tstart)); tdiag = tstart;
  *(LAS u32*)(L + threadIdx.x * 4) = SENT;
  if (wave == 0) *(LAS u32*)(L + L_T0) = t0;
  BARRIER();
#define HEAD_OF(x_, k_) ((((x_) & 1) ^ 1) * 8 + ((x_) >> 1) * 2 + (k_))
  const int h = HEAD_OF(x, j >> 4), s = j & 15;
  const int xs = x ^ (1 + 3 * (j >> 4));
  const int hc = HEAD_OF(xs, (j >> 3) & 1), rc = j & 7;
  const rsrc_t rs = mkrs(ws_, WS_END);
  const unsigned char* wop = (rbr ? woB : woA) + (u32)(hc * 256 + rc * 32) * 1152;
  if (wave == 0) {
    const u32 tag = __builtin_amdgcn_raw_buffer_load_b32(rs, WS_EPOCH, 0, SC1) + 1u;
    {
      const rsrc_t rcode = mkrs((const void*)(pc & ~(u64)255), CODE_BYTES);
      const u32 c0 = __builtin_amdgcn_raw_buffer_load_b32(rcode, lane * 128, CODE_BYTES - 8192, 0);
      asm volatile("" ::"v"(c0));
    }
    *(LAS u32*)(L + L_FLAG + 4) = tag;
    const u32 tagb = tag & 0xffu;
    STAMP(1);
    {
      u32 t[9];
#pragma unroll 1
      for (u32 spin = 0; spin < SPIN_CAP * 8; ++spin) {
#pragma unroll
        for (int k = 0; k < 9; ++k) t[k] = *(volatile LAS u32*)(L + L_QP + k * 256 + lane * 4);
        if (none(max(max(max(max(t[0], t[1]), max(t[2], t[3])), max(max(t[4], t[5]), max(t[6], t[7]))), t[8]) == SENT)) break;
        __builtin_amdgcn_s_sleep(1);
      }
      const float qp = ((asf(t[0]) + asf(t[1])) + (asf(t[2]) + asf(t[3]))) + ((asf(t[4]) + asf(t[5])) + (asf(t[6]) + asf(t[7]))) + asf(t[8]);
      __builtin_amdgcn_raw_buffer_store_b64(u32x2{__float_as_uint(qp), (tag & 0xffffu) | (t0 << 16)}, rs, WS_Q + (x * 32 + j) * 512 + lane * 8, 0, SC1);
    }
    STAMP(2);
    {
      u32x4 v[8];
      u32x2 tv;
      u32 spin = 0;
#pragma unroll 1
      for (; spin < SPIN_CAP; ++spin) {
        __builtin_amdgcn_s_sleep(QSLEEP);
        asm volatile("" ::: "memory");
#pragma unroll
        for (int i = 0; i < 8; ++i) v[i] = ldsc(rs, WS_Q + h * 2048 + lane * 16 + ((i >> 1) & 1) * 1024, ((i >> 2) * 2 + (i & 1)) * 32768);
        tv = __builtin_amdgcn_raw_buffer_load_b64(rs, WS_Q + (lane & 7) * 16384, 0, SC1);
        u32 bad = tv[1] ^ tag;
#pragma unroll
        for (int i = 0; i < 8; ++i) bad |= badtag(v[i], tag);
        if (none((bad & 0xffffu) != 0)) break;
      }
      STAMPV(22, spin + 1);
#pragma unroll
      for (int i = 0; i < 4; ++i)
        *(LAS u32*)(L + L_Q + i * 256 + lane * 4) = cvtpk(asf(v[2 * i][0]) + asf(v[2 * i + 1][0]), asf(v[2 * i][2]) + asf(v[2 * i + 1][2]));
      int d = (int)(tv[1] - (t0 << 16)) >> 16;
      d += dppi<XOR1>(d); d += dppi<XOR2>(d); d += dppi<HMIR>(d);
      d = __builtin_amdgcn_readfirstlane(d) >> 3;
      t0 += d > 300 ? 300 : d;
      *(LAS u32*)(L + L_T0) = t0;
      LGKWAIT();
      *(volatile LAS u32*)(L + L_FLAG) = 0;
    }
    STAMP(4);
    {
      u32x2 ml;
#pragma unroll 1
      for (u32 spin = 0; spin < SPIN_CAP * 8; ++spin) {
        ml = *(volatile LAS u32x2*)(L + L_ML + (lane < 20 ? lane : 0) * 8);
        if (none(max(ml[0], ml[1]) == SENT)) break;
        __builtin_amdgcn_s_sleep(2);
      }
      STAMP(6);
      const bool live = lane < 20;
      float m = live ? asf(ml[0]) : -INFINITY;
      float M = fmaxf(m, dpp<XOR2>(m)); M = fmaxf(M, dpp<ROR4>(M)); M = fmaxf(M, dpp<ROR8>(M));
      const float F = __builtin_amdgcn_exp2f(m - M);
      if (live) *(LAS float*)(L + L_F + lane * 4) = F;
      float l = live ? asf(ml[1]) * F : 0.f;
      l += dpp<XOR2>(l); l += dpp<ROR4>(l); l += dpp<ROR8>(l);
      __builtin_amdgcn_raw_buffer_store_b128(u32x4{__float_as_uint(M), tag, __float_as_uint(l), tag}, rs,
                                             (lane & 46) == 0 ? WS_ML + (h * 16 + s) * 128 + ((lane < 16 ? 2 : 0) + (lane & 1)) * 16 : WS_END, 0, SC1);
    }
    const int moff = WS_ML + (hc * 16 + (lane & 15)) * 128 + (rbr * 2 + (rc >> 2)) * 16;
    const int ioff = WS_O + (hc * 16 + rbr * 8 + rc) * 2048 + (lane & 7) * 128 + (lane >> 3) * 16;
    const int br2 = rbr ^ 1, rr = ((j >> 2) * 4 + (x & 3)) * 4 + (j & 3);
    const int yoff = WS_Y + (br2 * 128 + (lane >> 1)) * 2304 + rr * 32 + (lane & 1) * 16;
    const int outoff = (lane & 14) == 0 ? (br2 * 576 + rr * 8 + 4 * (lane & 1) + (lane >> 4)) * 2 : 4096;
    const rsrc_t rout = mkrs(out_, 2304);
    STAMP(7);
    until<slot_ns(33)>(t0);
    float w0, w1;
    {
      u32x4 gv;
#pragma unroll 1
      for (u32 spin = 0; spin < SPIN_CAP; ++spin) {
        asm volatile("" ::: "memory");
        gv = ldsc(rs, moff, 0);
        if (none(badtag(gv, tag) != 0)) break;
      }
      const float gm = asf(gv[0]);
      float GM = fmaxf(gm, dpp<XOR1>(gm)); GM = fmaxf(GM, dpp<XOR2>(GM)); GM = fmaxf(GM, dpp<HMIR>(GM)); GM = fmaxf(GM, dpp<ROR8>(GM));
      const float ex = __builtin_amdgcn_exp2f(gm - GM);
      float GL = asf(gv[2]) * ex;
      GL += dpp<XOR1>(GL); GL += dpp<XOR2>(GL); GL += dpp<HMIR>(GL); GL += dpp<ROR8>(GL);
      const float w = ex / GL, wx = dpp<ROR8>(w);
      w0 = (lane & 8) ? wx : w; w1 = (lane & 8) ? w : wx;
    }
    asm volatile("" ::"v"(w0), "v"(w1), "v"(yoff), "v"(ioff), "v"(outoff), "s"(rout));
    STAMP(8);
#pragma unroll 1
    for (u32 spin = 0; spin < SPIN_CAP * 8; ++spin) {
      const u32x2 c = *(volatile LAS u32x2*)(L + L_FLAG + 8);
      if ((int)(c[0] + c[1]) >= 8) break;
      __builtin_amdgcn_s_sleep(1);
    }
    until<slot_ns(35) + D1>(t0);
    STAMP(9);
    {
      u32x4 v0, v1;
      u32 spin = 0;
#pragma unroll 1
      for (; spin < SPIN_CAP; ++spin) {
        asm volatile("" ::: "memory");
        v0 = ldsc(rs, ioff, 0) ^ tagb;
        v1 = ldsc(rs, ioff + 1024, 0) ^ tagb;
        if (none(((or4(v0) | or4(v1)) & 0xffu) != 0)) break;
      }
      STAMP(10); STAMPV(20, spin + 1);
      f32x4 ov = asf4(v0) * w0 + asf4(v1) * w1;
      SB();
#pragma unroll
      for (int i = 0; i < 4; ++i) ov[i] += dpp<XOR1>(ov[i]);
      SB();
#pragma unroll
      for (int i = 0; i < 4; ++i) ov[i] += dpp<XOR2>(ov[i]);
      SB();
#pragma unroll
      for (int i = 0; i < 4; ++i) ov[i] += dpp<HMIR>(ov[i]);
      SB();
      const u32x4 e = {cvtpk(ov[0], ov[0]), cvtpk(ov[1], ov[1]), cvtpk(ov[2], ov[2]), cvtpk(ov[3], ov[3])};
      *(LAS u32x4*)(L + L_OF + (lane >> 3) * 16) = e;
    }
    BARRIER();
    STAMP(11);
    if (rr < 72) {
      __builtin_amdgcn_s_sleep(D2);
      u32x4 v[4];
      u32 spin = 0;
#pragma unroll 1
      for (; spin < SPIN_CAP; ++spin) {
        asm volatile("" ::: "memory");
#pragma unroll
        for (int i = 0; i < 4; ++i) v[i] = ldsc(rs, yoff, i * 32 * 2304) ^ tagb;
        if (none((((or4(v[0]) | or4(v[1])) | (or4(v[2]) | or4(v[3]))) & 0xffu) != 0)) break;
      }
      STAMP(12); STAMPV(21, spin + 1);
      const f32x4 y = (asf4(v[0]) + asf4(v[1])) + (asf4(v[2]) + asf4(v[3]));
      auto t0_ = __builtin_amdgcn_permlane32_swap(__float_as_uint(y[0]), __float_as_uint(y[2]), false, false);
      auto t1_ = __builtin_amdgcn_permlane32_swap(__float_as_uint(y[1]), __float_as_uint(y[3]), false, false);
      const float z0 = asf(t0_[0]) + asf(t0_[1]), z1 = asf(t1_[0]) + asf(t1_[1]);
      auto t2_ = __builtin_amdgcn_permlane16_swap(__float_as_uint(z0), __float_as_uint(z1), false, false);
      float z = asf(t2_[0]) + asf(t2_[1]);
      z += dpp<ROR8>(z); z += dpp<ROR4>(z); z += dpp<XOR2>(z);
      __builtin_amdgcn_raw_buffer_store_b16((unsigned short)cvtpk(z, z), rout, outoff, 0, SC1);
      if (b == 0) __builtin_amdgcn_raw_buffer_store_b32(tag, rs, lane == 0 ? WS_EPOCH : WS_END, 0, SC1);
    }
    STAMP(13);
    FINISH();
  }
  const u32 kvo = (u32)(h * 2048 + (br ? 0 : 1536 - 128) + a * 16 + (s ^ (MIX ? (a & 1) * 8 : 0))) * 1024;
  const u32 sh = br ? 17 : 15, kvb = (15u << sh) + 1024;
  const rsrc_t rk = mkrs((br ? kB : kA) + kvo, kvb);
  const rsrc_t rv = mkrs((br ? vB : vA) + kvo, kvb);
  const u32 koff = (u32)((lane >> 2) & 3) * 256 + (lane & 3) * 64 + (lane >> 4) * 16, voff = lane * 16;
#define CHUNK(j_) ((u32)((j_) ^ h) << sh)
#define ORIGIN() (__builtin_amdgcn_readfirstlane(*(volatile LAS u32*)(L + L_T0)) + (u32)(a * STAG) / 10u)
  u32x4 msk;
#pragma unroll
  for (int k = 0; k < 4; ++k) msk[k] = ((lane & 7) >> 1) == k ? ((lane & 1) ? 0xffff0000u : 0x0000ffffu) : 0u;
  const int colp = (lane & 7) * 8 + ((lane >> 4) & 1) * 4 + (lane >> 5) * 2;
  t0 = ORIGIN();
  SB();
  sfor<0, 16>([&](auto ic) {
    constexpr int i = decltype(ic)::v;
    if constexpr (i >= 8) t0 = ORIGIN();
    until<slot_ns(4 + i)>(t0);
    VMWAIT(i < 4 ? WIN : WIN - 1);
    K[i] = ldnt(rk, koff, CHUNK(i));
    SB();
    if constexpr (i == 2) {
      const u32x4 e = {__builtin_amdgcn_perm(xv[0], xv[0], 0x01000100u), __builtin_amdgcn_perm(xv[0], xv[0], 0x03020302u),
                       __builtin_amdgcn_perm(xv[1], xv[1], 0x01000100u), __builtin_amdgcn_perm(xv[1], xv[1], 0x03020302u)};
      const f32x2 qp = gemv_tile(e, msk, V);
      if (!(lane & 8) && a < 9) *(LAS f32x2*)(L + L_QP + a * 256 + colp * 4) = qp;
      SB();
      STAMP(1);
    }
  });
  const rsrc_t ro = mkrs(wop, a < 9 ? 32 * 1152 : 0);
  const u32 wooff = (u32)(4 * (lane >> 3)) * 1152 + (lane & 7) * 16 + a * 128;
#define ISSUE(n) do { if ((n) < 16) V[n] = ldnt(rv, voff, CHUNK(n)); else if ((n) < 20) V[n] = ldnt(ro, wooff + ((n) - 16) * 1152, 0); } while (0)
  float sc[16];
  u32 P[16];
  u32x4 Q;
  u32 tag;
  float m, l = 0.f, F;
  const float LOG2E = 1.4426950408889634f;
  const int yo = (lane & 8) ? WS_END : WS_Y + (rbr * 128 + hc * 8 + rc) * 2304 + (a * 64 + colp) * 4;
  const int ooff = WS_O + (h * 16 + br * 8 + (lane >> 5) * 4 + ((lane & 15) >> 2)) * 2048 + s * 128 + (lane & 3) * 32 + ((lane >> 4) & 1) * 16;
  LAS unsigned char* cntp = L + L_FLAG + 12 - br * 4;
  f32x4 o = {0.f, 0.f, 0.f, 0.f};
  sfor<0, 16 + WIN>([&](auto nc) {
    constexpr int n = decltype(nc)::v;
    u32x4 sel;
    if constexpr (n > QOFF + 6) { sel = msk & P[n - WIN]; asm volatile("" : "+v"(sel)); }
    SB();
    if constexpr (n <= QOFF + 1) t0 = ORIGIN();
    if constexpr (n < 16 + WOPRE) until<slot_ns(20 + n)>(t0);
    VMWAIT(issued_after(n - 1) - (n - WIN + 1));
    sfor<issued_after(n - 1), issued_after(n)>([&](auto kc) { constexpr int k = decltype(kc)::v; ISSUE(k); });
    SB();
    if constexpr (n == QOFF) {
      STAMP(2);
#pragma unroll 1
      for (u32 spin = 0; spin < SPIN_CAP * 8; ++spin) { if (*(volatile LAS u32*)(L + L_FLAG) == 0) break; __builtin_amdgcn_s_sleep(1); }
      asm volatile("" ::: "memory");
      STAMP(3);
      Q = *(LAS u32x4*)(L + L_Q + br * 512 + ((lane >> 3) & 1) * 256 + (lane & 3) * 64 + (lane >> 4) * 16);
      tag = *(LAS u32*)(L + L_FLAG + 4);
    }
    if constexpr (n < QOFF) {
    } else if constexpr (n < QOFF + 4) {
      constexpr int T = n - QOFF;
      float d[4];
#pragma unroll
      for (int u = 0; u < 4; ++u) {
        const f32x4 t = mfma(K[T * 4 + u], Q, f32x4{0.f, 0.f, 0.f, 0.f});
        d[u] = ((dpp<0x00>(t[0]) + dpp<0x55>(t[1])) + dpp<0xAA>(t[2])) + dpp<0xFF>(t[3]);
      }
      const u32 r0 = cvtpk(d[0], d[1]), r1 = cvtpk(d[2], d[3]);
      const float SC = 0.08838834764831845f;
      const u32 r2 = cvtpk(bflo(r0) * SC, bfhi(r0) * SC), r3 = cvtpk(bflo(r1) * SC, bfhi(r1) * SC);
      sc[T * 4 + 0] = bflo(r2); sc[T * 4 + 1] = bfhi(r2); sc[T * 4 + 2] = bflo(r3); sc[T * 4 + 3] = bfhi(r3);
    } else if constexpr (n == QOFF + 4) {
      m = sc[0];
#pragma unroll
      for (int i = 1; i < 16; ++i) m = fmaxf(m, sc[i]);
      m = x16_32_max(m);
#pragma unroll
      for (int i = 0; i < 6; ++i) { const float p = __builtin_amdgcn_exp2f((sc[i] - m) * LOG2E); l += p; P[i] = cvtpk(p, p); }
    } else if constexpr (n == QOFF + 5) {
#pragma unroll
      for (int i = 6; i < 16; ++i) { const float p = __builtin_amdgcn_exp2f((sc[i] - m) * LOG2E); l += p; P[i] = cvtpk(p, p); }
      l = x16_32_sum(l);
      if ((lane & 55) == 0) *(LAS f32x2*)(L + L_ML + a * 16 + (lane >> 3) * 8) = f32x2{m * LOG2E, l};
      asm volatile("" ::"v"(yo), "v"(ooff), "v"(cntp));
      STAMP(4);
    } else if constexpr (n == QOFF + 6) {
#pragma unroll
      for (int i = 0; i <= n - WIN; ++i) o = mfma(msk & P[i], V[i], o);
    } else {
      o = mfma(sel, V[n - WIN], o);
      asm volatile("" : "+v"(o));
      if constexpr (n == 14) {
#pragma unroll 1
        for (u32 spin = 0; spin < SPIN_CAP * 8; ++spin) {
          const u32 f = *(volatile LAS u32*)(L + L_F + a * 8 + (lane >> 5) * 4);
          F = __uint_as_float(f);
          if (none(f == SENT)) break;
          __builtin_amdgcn_s_sleep(1);
        }
      }
    }
    SB();
  });
  *(LAS f32x4*)(L + L_O + a * 1024 + lane * 16) = o * F;
  u32 arr;
  asm volatile("s_mov_b64 exec, 1\n\tds_add_rtn_u32 %0, %1, %2\n\ts_mov_b64 exec, -1\n\ts_waitcnt lgkmcnt(0)"
               : "=&v"(arr) : "v"(cntp), "v"(1u) : "memory");
  arr = __builtin_amdgcn_readfirstlane(arr);
  STAMP(5);
  if (arr == (br ? 6u : 0u)) {
    f32x4 t;
    if (br) {
      f32x4 u[8];
#pragma unroll
      for (int k = 0; k < 8; ++k) u[k] = *(LAS f32x4*)(L + L_O + k * 1024 + lane * 16);
      t = ((u[0] + u[1]) + (u[2] + u[3])) + ((u[4] + u[5]) + (u[6] + u[7]));
    } else {
      t = *(LAS f32x4*)(L + L_O + 8 * 1024 + lane * 16) + *(LAS f32x4*)(L + L_O + 9 * 1024 + lane * 16);
    }
    __builtin_amdgcn_raw_buffer_store_b128(u32x4{tw(t[0], tag), tw(t[1], tag), tw(t[2], tag), tw(t[3], tag)}, rs, ooff, 0, SC1);
    STAMP(8);
  }
  if (a == 9) FINISH();
  sfor<16 + WOPRE, 20>([&](auto kc) { constexpr int k = decltype(kc)::v; until<slot_ns(20 + k)>(t0); ISSUE(k); });
  SB();
  BARRIER();
  const u32x4 e = *(LAS u32x4*)(L + L_OF + (lane >> 3) * 16);
  const f32x2 y = gemv_tile(e, msk, V + 16);
  __builtin_amdgcn_raw_buffer_store_b64(u32x2{tw(y[0], tag), tw(y[1], tag)}, rs, yo, 0, SC1);
  STAMP(7);
  FINISH();
}

extern "C" void kernel_launch(void* const* d_in, const int* in_sizes, int n_in, void* d_out, int out_size, void* d_ws,
                              size_t ws_size, hipStream_t stream) {
  typedef const unsigned char* P;
  fused_decode<<<256, NTHR, 0, stream>>>((P)d_in[0], (P)d_in[1], (P)d_in[2], (P)d_in[3], (P)d_in[4], (P)d_in[5], (P)d_in[6],
                                         (P)d_in[7], (P)d_in[8], (P)d_in[9], (unsigned char*)d_out, (unsigned char*)d_ws);
}
```

```cpp
#define PACE 338
#define PT0 190
#define STAG 33
#define CODEB 8600
#define AUXO 0
#define DS 13
#define DW 0
#define DK 0
#define DM 5
#define DV 0
#include <hip/hip_runtime.h>
#include <hip/hip_bf16.h>
#include <stdint.h>

#define STAMPS 0
#define WIN 4
#define QSLEEP 16
#define QOFF 1
#define PWO PACE
#define WOPRE 1
#define D1 1500
#define D2 1150
#define TQ 4800
#define NOCOMM 0
#define NOWQ 0
#define HX h
#define NOCOMP 0
#define AUXQ 2
#define AUXK 2
#define AUXV 2
#define MIX 1
#define TOUCH 0

typedef unsigned int u32;
typedef unsigned long long u64;
typedef u32 u32x2 __attribute__((ext_vector_type(2)));
typedef u32 u32x4 __attribute__((ext_vector_type(4)));
typedef float f32x2 __attribute__((ext_vector_type(2)));
typedef float f32x4 __attribute__((ext_vector_type(4)));
typedef __bf16 bf16x2 __attribute__((ext_vector_type(2)));
typedef __bf16 bf16x8 __attribute__((ext_vector_type(8)));
#define GAS __attribute__((address_space(1)))
#define LAS __attribute__((address_space(3)))
typedef __amdgpu_buffer_rsrc_t rsrc_t;

constexpr int NWAVE = 11, NTHR = NWAVE * 64;
constexpr u32 SENT = 0xFFFFFFFFu;
constexpr u32 SPIN_CAP = 1u << 17;
constexpr int WS_EPOCH = 0;
constexpr int WS_Q = 4096;
constexpr int WS_ML = WS_Q + 256 * 512;
constexpr int WS_O = WS_ML + 256 * 128;
constexpr int WS_Y = WS_O + 256 * 2048;
constexpr int WS_END = WS_Y + 256 * 2304;
constexpr int WS_STAMP = 4 << 20;
constexpr int L_QP = 0;
constexpr int L_ML = 2304;
constexpr int L_F = L_ML + 160;
constexpr int L_FLAG = L_F + 80;
constexpr int L_T0 = 2816;
constexpr int L_OF = 2944;
constexpr int L_Q = 3072;
constexpr int L_O = 4096;
constexpr int L_BYTES = L_O + 10 * 1024;
constexpr int SC1 = 16, NT = 2;
constexpr u32 CODE_BYTES = CODEB;

__device__ __forceinline__ rsrc_t mkrs(const void* p, u32 bytes) { return __builtin_amdgcn_make_buffer_rsrc((void*)p, 0, bytes, 0x00020000); }
__device__ __forceinline__ u32x4 ldnt(rsrc_t r, u32 voff, u32 soff) { return __builtin_amdgcn_raw_buffer_load_b128(r, voff, soff, NT); }
__device__ __forceinline__ u32x4 ldsc(rsrc_t r, u32 voff, u32 soff) { return __builtin_amdgcn_raw_buffer_load_b128(r, voff, soff, SC1); }
__device__ __forceinline__ u32 badtag(u32x4 v, u32 tag) { return (v[1] ^ tag) | (v[3] ^ tag); }
__device__ __forceinline__ u32 tw(float x, u32 tag) { return __builtin_amdgcn_perm(__float_as_uint(x), tag, 0x07060500u); }
__device__ __forceinline__ u32 or4(u32x4 v) { return (v[0] | v[1]) | (v[2] | v[3]); }
__device__ __forceinline__ f32x4 mfma(u32x4 a, u32x4 b, f32x4 c) {
  return __builtin_amdgcn_mfma_f32_16x16x32_bf16(__builtin_bit_cast(bf16x8, a), __builtin_bit_cast(bf16x8, b), c, 0, 0, 0);
}
__device__ __forceinline__ u32 cvtpk(float lo, float hi) {
  bf16x2 r = {(__bf16)lo, (__bf16)hi};
  return __builtin_bit_cast(u32, r);
}
__device__ __forceinline__ float bflo(u32 x) { return __uint_as_float(x << 16); }
__device__ __forceinline__ float bfhi(u32 x) { return __uint_as_float(x & 0xffff0000u); }
__device__ __forceinline__ float asf(u32 x) { return __uint_as_float(x); }
__device__ __forceinline__ f32x4 asf4(u32x4 x) { return __builtin_bit_cast(f32x4, x); }
template <int C> __device__ __forceinline__ float dpp(float x) {
  return __builtin_bit_cast(float, __builtin_amdgcn_mov_dpp(__builtin_bit_cast(int, x), C, 0xf, 0xf, true));
}
template <int C> __device__ __forceinline__ int dppi(int x) { return __builtin_amdgcn_mov_dpp(x, C, 0xf, 0xf, true); }
constexpr int XOR1 = 0xB1, XOR2 = 0x4E, HMIR = 0x141, ROR8 = 0x128, ROR4 = 0x124;
__device__ __forceinline__ float x16_32_max(float x) {
  auto s = __builtin_amdgcn_permlane16_swap(__float_as_uint(x), __float_as_uint(x), false, false);
  x = fmaxf(__uint_as_float(s[0]), __uint_as_float(s[1]));
  auto t = __builtin_amdgcn_permlane32_swap(__float_as_uint(x), __float_as_uint(x), false, false);
  return fmaxf(__uint_as_float(t[0]), __uint_as_float(t[1]));
}
__device__ __forceinline__ float x16_32_sum(float x) {
  auto s = __builtin_amdgcn_permlane16_swap(__float_as_uint(x), __float_as_uint(x), false, false);
  x = __uint_as_float(s[0]) + __uint_as_float(s[1]);
  auto t = __builtin_amdgcn_permlane32_swap(__float_as_uint(x), __float_as_uint(x), false, false);
  return __uint_as_float(t[0]) + __uint_as_float(t[1]);
}
__device__ __forceinline__ bool none(bool p) { return __builtin_amdgcn_ballot_w64(p) == 0; }
__device__ __forceinline__ f32x2 gemv_tile(u32x4 e, u32x4 msk, const u32x4* w) {
  f32x4 y = {0.f, 0.f, 0.f, 0.f};
#pragma unroll
  for (int i = 0; i < 4; ++i) y = mfma(msk & e[i], w[i], y);
  auto t0 = __builtin_amdgcn_permlane32_swap(__float_as_uint(y[0]), __float_as_uint(y[2]), false, false);
  auto t1 = __builtin_amdgcn_permlane32_swap(__float_as_uint(y[1]), __float_as_uint(y[3]), false, false);
  return f32x2{__uint_as_float(t0[0]) + dpp<ROR8>(__uint_as_float(t0[1])), __uint_as_float(t1[0]) + dpp<ROR8>(__uint_as_float(t1[1]))};
}
template <int I> struct IC { static constexpr int v = I; };
template <int LO, int HI, class Fn> __device__ __forceinline__ void sfor(Fn&& f) {
  if constexpr (LO < HI) { f(IC<LO>{}); sfor<LO + 1, HI>(f); }
}
constexpr int issued_after(int n) { return n < 0 ? 0 : (n + 1 < 16 + WOPRE ? n + 1 : 16 + WOPRE); }
constexpr int slot_ns(int g) { return PT0 + (g <= 36 ? g * PACE : 36 * PACE + (g - 36) * PWO); }
#define SB() __builtin_amdgcn_sched_barrier(0)
#define VMWAIT(n) asm volatile("s_waitcnt vmcnt(%0)" ::"n"(n) : "memory")
#define LGKWAIT() asm volatile("s_waitcnt lgkmcnt(0)" ::: "memory")
#define BARRIER() asm volatile("s_waitcnt lgkmcnt(0)\n\ts_barrier" ::: "memory")
template <int NS> __device__ __forceinline__ void until(u32 t0) {
  asm volatile("1:\n\ts_memrealtime vcc\n\ts_waitcnt lgkmcnt(0)\n\ts_sub_u32 vcc_lo, vcc_lo, %0\n\ts_cmp_lt_i32 vcc_lo, %1\n\ts_cbranch_scc1 1b"
               : : "s"(t0), "n"(NS / 10) : "memory", "scc", "vcc");
}
#define RT(x) asm volatile("s_memrealtime %0\n\ts_waitcnt lgkmcnt(0)" : "=s"(x)::"memory")
#define STAMP(i) do { if ((i) == DS && wave == DW) { if (DV) VMWAIT(0); RT(tdiag); } } while (0)
#define STAMPV(i, v) do {} while (0)
#define FINISH() do { GAS u64* sp_ = (GAS u64*)((GAS unsigned char*)ws_ + (6 << 20)); \
  if (wave == DW && lane == 0) { __hip_atomic_store(sp_ + b, tstart, __ATOMIC_RELAXED, __HIP_MEMORY_SCOPE_AGENT); __hip_atomic_store(sp_ + 256 + b, tdiag, __ATOMIC_RELAXED, __HIP_MEMORY_SCOPE_AGENT); } \
  if (wave == 0 && b == 0) { u64 t; do { __builtin_amdgcn_s_sleep(4); RT(t); } while (t < tstart + 3500); \
    u64 mn = ~0ull, mx = 0, dn = ~0ull, sum = 0; for (int i = 0; i < 4; ++i) { u64 v = __hip_atomic_load(sp_ + i * 64 + lane, __ATOMIC_RELAXED, __HIP_MEMORY_SCOPE_AGENT); u64 d = __hip_atomic_load(sp_ + 256 + i * 64 + lane, __ATOMIC_RELAXED, __HIP_MEMORY_SCOPE_AGENT); mn = v < mn ? v : mn; mx = d > mx ? d : mx; dn = d < dn ? d : dn; sum += d; } \
    for (int m_ = 1; m_ < 64; m_ <<= 1) { u64 o1 = __shfl_xor(mn, m_), o2 = __shfl_xor(mx, m_), o3 = __shfl_xor(dn, m_), o4 = __shfl_xor(sum, m_); mn = o1 < mn ? o1 : mn; mx = o2 > mx ? o2 : mx; dn = o3 < dn ? o3 : dn; sum += o4; } \
    u64 xv_ = DK == 0 ? mx - mn : DK == 1 ? dn - mn : sum / 256 - mn; if (xv_ > 4000) xv_ = 4000; \
    do { __builtin_amdgcn_s_sleep(4); RT(t); } while (t < mn + 5000 + DM * xv_); } __builtin_amdgcn_endpgm(); } while (0)

__global__ void __launch_bounds__(NTHR, 3)
fused_decode(const unsigned char* xA, const unsigned char* kA, const unsigned char* vA, const unsigned char* wqA,
             const unsigned char* woA, const unsigned char* xB, const unsigned char* kB, const unsigned char* vB,
             const unsigned char* wqB, const unsigned char* woB, unsigned char* out_, unsigned char* ws_) {
  __shared__ __attribute__((aligned(16))) unsigned char smem[L_BYTES];
  LAS unsigned char* L = (LAS unsigned char*)smem;
  u64 tstart, tdiag;
  asm volatile("s_memrealtime %0" : "=s"(tstart));
  const int b = blockIdx.x, lane = threadIdx.x & 63;
  const int wave = __builtin_amdgcn_readfirstlane(threadIdx.x >> 6);
  const int x = b & 7, j = b >> 3;
  const int a = wave - 1, br = a < 8;
  const int rbr = x >> 2, rh = (x >> 1) & 1, cb = (x & 1) * 32 + j;
  const int row = rh * 288 + a * 32 + 4 * (lane >> 3);
  const u32 qoff = (u32)row * 8192 + cb * 128 + (lane & 7) * 16;
  const u32 nq = (u32)a < 9u && !NOWQ ? 576 * 8192 : 0, nx = (u32)a < 9u ? 1152 : 0;
  asm volatile("" ::"v"(qoff), "v"(row * 2), "s"(nq), "s"(nx));
  SB();
  asm volatile("" ::"s"(xA), "s"(kA), "s"(vA), "s"(wqA), "s"(woA), "s"(xB), "s"(kB), "s"(vB), "s"(wqB), "s"(woB), "s"(out_), "s"(ws_));
  u32x4 K[16], V[20];
  const rsrc_t rq = mkrs(rbr ? wqB : wqA, nq);
#pragma unroll
  for (int i = 0; i < 4; ++i) V[i] = __builtin_amdgcn_raw_buffer_load_b128(rq, qoff, i * 8192, AUXQ);
  const u32x2 xv = __builtin_amdgcn_raw_buffer_load_b64(mkrs(rbr ? xB : xA, nx), row * 2, 0, 0);
  SB();
  u32 t0 = (u32)__builtin_amdgcn_s_memrealtime();
  u64 pc;
  asm volatile("s_getpc_b64 %0" : "=s"(pc));
  asm volatile("s_waitcnt lgkmcnt(0)" : "+s"(tstart)); tdiag = tstart;
  *(LAS u32*)(L + threadIdx.x * 4) = SENT;
  if (wave == 0) *(LAS u32*)(L + L_T0) = t0;
  BARRIER();
#define HEAD_OF(x_, k_) ((((x_) & 1) ^ 1) * 8 + ((x_) >> 1) * 2 + (k_))
  const int h = HEAD_OF(x, j >> 4), s = j & 15;
  const int xs = x ^ (1 + 3 * (j >> 4));
  const int hc = HEAD_OF(xs, (j >> 3) & 1), rc = j & 7;
  const rsrc_t rs = mkrs(ws_, WS_END);
  const unsigned char* wop = (rbr ? woB : woA) + (u32)(hc * 256 + rc * 32) * 1152;
  if (wave == 0) {
    const u32 tag = __builtin_amdgcn_raw_buffer_load_b32(rs, WS_EPOCH, 0, SC1) + 1u;
    {
      const rsrc_t rcode = mkrs((const void*)(pc & ~(u64)255), CODE_BYTES);
      const u32 c0 = __builtin_amdgcn_raw_buffer_load_b32(rcode, lane * 128, CODE_BYTES - 8192, 0);
      asm volatile("" ::"v"(c0));
    }
    *(LAS u32*)(L + L_FLAG + 4) = tag;
    const u32 tagb = tag & 0xffu;
    STAMP(1);
    {
      u32 t[9];
#pragma unroll 1
      for (u32 spin = 0; spin < SPIN_CAP * 8; ++spin) {
#pragma unroll
        for (int k = 0; k < 9; ++k) t[k] = *(volatile LAS u32*)(L + L_QP + k * 256 + lane * 4);
        if (none(max(max(max(max(t[0], t[1]), max(t[2], t[3])), max(max(t[4], t[5]), max(t[6], t[7]))), t[8]) == SENT)) break;
        __builtin_amdgcn_s_sleep(1);
      }
      const float qp = ((asf(t[0]) + asf(t[1])) + (asf(t[2]) + asf(t[3]))) + ((asf(t[4]) + asf(t[5])) + (asf(t[6]) + asf(t[7]))) + asf(t[8]);
      __builtin_amdgcn_raw_buffer_store_b64(u32x2{__float_as_uint(qp), (tag & 0xffffu) | (t0 << 16)}, rs, WS_Q + (x * 32 + j) * 512 + lane * 8, 0, SC1);
    }
    STAMP(2);
    {
      u32x4 v[8];
      u32x2 tv;
      u32 spin = 0;
      until<TQ>(t0);
#pragma unroll 1
      for (; spin < SPIN_CAP; ++spin) {
        asm volatile("" ::: "memory");
#pragma unroll
        for (int i = 0; i < 8; ++i) v[i] = ldsc(rs, WS_Q + h * 2048 + lane * 16 + ((i >> 1) & 1) * 1024, ((i >> 2) * 2 + (i & 1)) * 32768);
        tv = __builtin_amdgcn_raw_buffer_load_b64(rs, (lane & 7) == x ? WS_END : WS_Q + (lane & 7) * 16384 + 31 * 512, 0, SC1);
        if ((lane & 7) == x) tv[1] = (tag & 0xffffu) | (t0 << 16);
        u32 bad = tv[1] ^ tag;
#pragma unroll
        for (int i = 0; i < 8; ++i) bad |= badtag(v[i], tag);
        if (none((bad & 0xffffu) != 0)) break;
        __builtin_amdgcn_s_sleep(QSLEEP);
      }
      STAMPV(22, spin + 1);
#pragma unroll
      for (int i = 0; i < 4; ++i)
        *(LAS u32*)(L + L_Q + i * 256 + lane * 4) = cvtpk(asf(v[2 * i][0]) + asf(v[2 * i + 1][0]), asf(v[2 * i][2]) + asf(v[2 * i + 1][2]));
      int d = (int)(tv[1] - (t0 << 16)) >> 16;
      d = max(d, dppi<XOR1>(d)); d = max(d, dppi<XOR2>(d)); d = max(d, dppi<HMIR>(d));
      d = __builtin_amdgcn_readfirstlane(d);
      t0 += d > 300 ? 300 : d;
      STAMPV(16, t0);
      LGKWAIT();
      *(volatile LAS u32*)(L + L_FLAG) = 0;
    }
    STAMP(4);
    {
      u32x2 ml;
#pragma unroll 1
      for (u32 spin = 0; spin < SPIN_CAP * 8; ++spin) {
        ml = *(volatile LAS u32x2*)(L + L_ML + (lane < 20 ? lane : 0) * 8);
        if (none(max(ml[0], ml[1]) == SENT)) break;
        __builtin_amdgcn_s_sleep(2);
      }
      STAMP(6);
      const bool live = lane < 20;
      float m = live ? asf(ml[0]) : -INFINITY;
      float M = fmaxf(m, dpp<XOR2>(m)); M = fmaxf(M, dpp<ROR4>(M)); M = fmaxf(M, dpp<ROR8>(M));
      const float F = __builtin_amdgcn_exp2f(m - M);
      if (live) *(LAS float*)(L + L_F + lane * 4) = F;
      float l = live ? asf(ml[1]) * F : 0.f;
      l += dpp<XOR2>(l); l += dpp<ROR4>(l); l += dpp<ROR8>(l);
      __builtin_amdgcn_raw_buffer_store_b128(u32x4{__float_as_uint(M), tag, __float_as_uint(l), tag}, rs,
                                             (lane & 46) == 0 ? WS_ML + (h * 16 + s) * 128 + ((lane < 16 ? 2 : 0) + (lane & 1)) * 16 : WS_END, 0, SC1);
    }
    const int moff = WS_ML + (hc * 16 + (lane & 15)) * 128 + (rbr * 2 + (rc >> 2)) * 16;
    const int ioff = WS_O + (hc * 16 + rbr * 8 + rc) * 2048 + (lane & 7) * 128 + (lane >> 3) * 16;
    const int br2 = rbr ^ 1, rr = ((j >> 2) * 4 + (x & 3)) * 4 + (j & 3);
    const int yoff = WS_Y + (br2 * 128 + (lane >> 1)) * 2304 + rr * 32 + (lane & 1) * 16;
    const int outoff = (lane & 14) == 0 ? (br2 * 576 + rr * 8 + 4 * (lane & 1) + (lane >> 4)) * 2 : 4096;
    const rsrc_t rout = mkrs(out_, 2304);
    STAMP(7);
    until<slot_ns(32)>(t0);
    float w0, w1;
    {
      u32x4 gv;
#pragma unroll 1
      for (u32 spin = 0; spin < SPIN_CAP; ++spin) {
        asm volatile("" ::: "memory");
        gv = ldsc(rs, moff, 0);
        if (none(badtag(gv, tag) != 0)) break;
      }
      const float gm = asf(gv[0]);
      float GM = fmaxf(gm, dpp<XOR1>(gm)); GM = fmaxf(GM, dpp<XOR2>(GM)); GM = fmaxf(GM, dpp<HMIR>(GM)); GM = fmaxf(GM, dpp<ROR8>(GM));
      const float ex = __builtin_amdgcn_exp2f(gm - GM);
      float GL = asf(gv[2]) * ex;
      GL += dpp<XOR1>(GL); GL += dpp<XOR2>(GL); GL += dpp<HMIR>(GL); GL += dpp<ROR8>(GL);
      const float w = ex / GL, wx = dpp<ROR8>(w);
      w0 = (lane & 8) ? wx : w; w1 = (lane & 8) ? w : wx;
    }
    asm volatile("" ::"v"(w0), "v"(w1), "v"(yoff), "v"(ioff), "v"(outoff), "s"(rout));
    STAMP(8);
#pragma unroll 1
    for (u32 spin = 0; spin < SPIN_CAP * 8; ++spin) {
      const u32x2 c = *(volatile LAS u32x2*)(L + L_FLAG + 8);
      if ((int)(c[0] + c[1]) >= 8) break;
      __builtin_amdgcn_s_sleep(1);
    }
    until<slot_ns(35) + D1>(t0);
    STAMP(9);
    {
      u32x4 v0, v1;
      u32 spin = 0;
#pragma unroll 1
      for (; spin < SPIN_CAP; ++spin) {
        asm volatile("" ::: "memory");
        v0 = ldsc(rs, ioff, 0) ^ tagb;
        v1 = ldsc(rs, ioff + 1024, 0) ^ tagb;
        if (none(((or4(v0) | or4(v1)) & 0xffu) != 0)) break;
      }
      STAMP(10); STAMPV(20, spin + 1);
      f32x4 ov = asf4(v0) * w0 + asf4(v1) * w1;
      SB();
#pragma unroll
      for (int i = 0; i < 4; ++i) ov[i] += dpp<XOR1>(ov[i]);
      SB();
#pragma unroll
      for (int i = 0; i < 4; ++i) ov[i] += dpp<XOR2>(ov[i]);
      SB();
#pragma unroll
      for (int i = 0; i < 4; ++i) ov[i] += dpp<HMIR>(ov[i]);
      SB();
      const u32x4 e = {cvtpk(ov[0], ov[0]), cvtpk(ov[1], ov[1]), cvtpk(ov[2], ov[2]), cvtpk(ov[3], ov[3])};
      *(LAS u32x4*)(L + L_OF + (lane >> 3) * 16) = e;
    }
    BARRIER();
    STAMP(11);
    if (rr < 72) {
      until<slot_ns(35) + D1 + D2>(t0);
      u32x4 v[4];
      u32 spin = 0;
#pragma unroll 1
      for (; spin < SPIN_CAP; ++spin) {
        asm volatile("" ::: "memory");
#pragma unroll
        for (int i = 0; i < 4; ++i) v[i] = ldsc(rs, yoff, i * 32 * 2304) ^ tagb;
        if (none((((or4(v[0]) | or4(v[1])) | (or4(v[2]) | or4(v[3]))) & 0xffu) != 0)) break;
      }
      STAMP(12); STAMPV(21, spin + 1);
      const f32x4 y = (asf4(v[0]) + asf4(v[1])) + (asf4(v[2]) + asf4(v[3]));
      auto t0_ = __builtin_amdgcn_permlane32_swap(__float_as_uint(y[0]), __float_as_uint(y[2]), false, false);
      auto t1_ = __builtin_amdgcn_permlane32_swap(__float_as_uint(y[1]), __float_as_uint(y[3]), false, false);
      const float z0 = asf(t0_[0]) + asf(t0_[1]), z1 = asf(t1_[0]) + asf(t1_[1]);
      auto t2_ = __builtin_amdgcn_permlane16_swap(__float_as_uint(z0), __float_as_uint(z1), false, false);
      float z = asf(t2_[0]) + asf(t2_[1]);
      z += dpp<ROR8>(z); z += dpp<ROR4>(z); z += dpp<XOR2>(z);
      __builtin_amdgcn_raw_buffer_store_b16((unsigned short)cvtpk(z, z), rout, outoff, 0, SC1);
      if (b == 0) __builtin_amdgcn_raw_buffer_store_b32(tag, rs, lane == 0 ? WS_EPOCH : WS_END, 0, SC1);
    }
    STAMP(13);
    FINISH();
  }
  const u32 kvo = (u32)(h * 2048 + (br ? 0 : 1536 - 128) + a * 16 + (s ^ (MIX ? (a & 1) * 8 : 0))) * 1024;
  const u32 sh = br ? 17 : 15, kvb = (15u << sh) + 1024;
  const rsrc_t rk = mkrs((br ? kB : kA) + kvo, kvb);
  const rsrc_t rv = mkrs((br ? vB : vA) + kvo, kvb);
  const u32 koff = (u32)((lane >> 2) & 3) * 256 + (lane & 3) * 64 + (lane >> 4) * 16, voff = lane * 16;
  const int hx = HX;
#define CHUNK(j_) ((u32)((j_) ^ hx) << sh)
#define ORIGIN() (__builtin_amdgcn_readfirstlane(*(volatile LAS u32*)(L + L_T0)) + (u32)((MIX == 2 ? a & ~3 : a) * STAG) / 10u)
  u32x4 msk;
#pragma unroll
  for (int k = 0; k < 4; ++k) msk[k] = ((lane & 7) >> 1) == k ? ((lane & 1) ? 0xffff0000u : 0x0000ffffu) : 0u;
  const int colp = (lane & 7) * 8 + ((lane >> 4) & 1) * 4 + (lane >> 5) * 2;
  t0 = ORIGIN();
  SB();
  sfor<0, 16>([&](auto ic) {
    constexpr int i = decltype(ic)::v;
    until<slot_ns(4 + i)>(t0);
    VMWAIT(i < 4 ? WIN : WIN - 1);
    K[i] = __builtin_amdgcn_raw_buffer_load_b128(rk, koff, CHUNK(i), AUXK);
    SB();
    if constexpr (i == 2) {
      const u32x4 e = {__builtin_amdgcn_perm(xv[0], xv[0], 0x01000100u), __builtin_amdgcn_perm(xv[0], xv[0], 0x03020302u),
                       __builtin_amdgcn_perm(xv[1], xv[1], 0x01000100u), __builtin_amdgcn_perm(xv[1], xv[1], 0x03020302u)};
      const f32x2 qp = gemv_tile(e, msk, V);
      if (!(lane & 8) && a < 9) *(LAS f32x2*)(L + L_QP + a * 256 + colp * 4) = qp;
      SB();
      STAMP(1);
    }
  });
  const rsrc_t ro = mkrs(wop, a < 9 ? 32 * 1152 : 0);
  const u32 wooff = (u32)(4 * (lane >> 3)) * 1152 + (lane & 7) * 16 + a * 128;
#define ISSUE(n) do { if ((n) < 16) V[n] = __builtin_amdgcn_raw_buffer_load_b128(rv, voff, CHUNK(n), AUXV); else if ((n) < 20) V[n] = __builtin_amdgcn_raw_buffer_load_b128(ro, wooff + ((n) - 16) * 1152, 0, AUXO); } while (0)
  float sc[16];
  u32 P[16];
  u32x4 Q;
  u32 tag;
  float m, l = 0.f, F;
  const float LOG2E = 1.4426950408889634f;
  const int yo = (lane & 8) ? WS_END : WS_Y + (rbr * 128 + hc * 8 + rc) * 2304 + (a * 64 + colp) * 4;
  const int ooff = WS_O + (h * 16 + br * 8 + (lane >> 5) * 4 + ((lane & 15) >> 2)) * 2048 + s * 128 + (lane & 3) * 32 + ((lane >> 4) & 1) * 16;
  LAS unsigned char* cntp = L + L_FLAG + 12 - br * 4;
  f32x4 o = {0.f, 0.f, 0.f, 0.f};
  sfor<0, 16 + WIN>([&](auto nc) {
    constexpr int n = decltype(nc)::v;
    u32x4 sel;
    if constexpr (n > QOFF + 6) { sel = msk & P[n - WIN]; asm volatile("" : "+v"(sel)); }
    SB();
    if constexpr (n < 16 + WOPRE) until<slot_ns(20 + n)>(t0);
    VMWAIT(issued_after(n - 1) - (n - WIN + 1));
    sfor<issued_after(n - 1), issued_after(n)>([&](auto kc) { constexpr int k = decltype(kc)::v; ISSUE(k); });
    SB();
    if constexpr (n == QOFF) {
      STAMP(2);
#pragma unroll 1
      for (u32 spin = 0; spin < SPIN_CAP * 8; ++spin) { if (*(volatile LAS u32*)(L + L_FLAG) == 0) break; __builtin_amdgcn_s_sleep(1); }
      asm volatile("" ::: "memory");
      STAMP(3);
      Q = *(LAS u32x4*)(L + L_Q + br * 512 + ((lane >> 3) & 1) * 256 + (lane & 3) * 64 + (lane >> 4) * 16);
      tag = *(LAS u32*)(L + L_FLAG + 4);
    }
    if constexpr (n < QOFF) {
    } else if constexpr (NOCOMP && n < QOFF + 6) {
      if constexpr (n == QOFF + 5) {
#pragma unroll
        for (int i = 0; i < 16; ++i) { P[i] = K[i][0]; asm volatile("" ::"v"(K[i])); }
        STAMP(4);
      }
    } else if constexpr (n < QOFF + 4) {
      constexpr int T = n - QOFF;
      float d[4];
#pragma unroll
      for (int u = 0; u < 4; ++u) {
        const f32x4 t = mfma(K[T * 4 + u], Q, f32x4{0.f, 0.f, 0.f, 0.f});
        d[u] = ((dpp<0x00>(t[0]) + dpp<0x55>(t[1])) + dpp<0xAA>(t[2])) + dpp<0xFF>(t[3]);
      }
      const u32 r0 = cvtpk(d[0], d[1]), r1 = cvtpk(d[2], d[3]);
      const float SC = 0.08838834764831845f;
      const u32 r2 = cvtpk(bflo(r0) * SC, bfhi(r0) * SC), r3 = cvtpk(bflo(r1) * SC, bfhi(r1) * SC);
      sc[T * 4 + 0] = bflo(r2); sc[T * 4 + 1] = bfhi(r2); sc[T * 4 + 2] = bflo(r3); sc[T * 4 + 3] = bfhi(r3);
    } else if constexpr (n == QOFF + 4) {
      m = sc[0];
#pragma unroll
      for (int i = 1; i < 16; ++i) m = fmaxf(m, sc[i]);
      m = x16_32_max(m);
#pragma unroll
      for (int i = 0; i < 6; ++i) { const float p = __builtin_amdgcn_exp2f((sc[i] - m) * LOG2E); l += p; P[i] = cvtpk(p, p); }
    } else if constexpr (n == QOFF + 5) {
#pragma unroll
      for (int i = 6; i < 16; ++i) { const float p = __builtin_amdgcn_exp2f((sc[i] - m) * LOG2E); l += p; P[i] = cvtpk(p, p); }
      l = x16_32_sum(l);
      if ((lane & 55) == 0) *(LAS f32x2*)(L + L_ML + a * 16 + (lane >> 3) * 8) = f32x2{m * LOG2E, l};
      asm volatile("" ::"v"(yo), "v"(ooff), "v"(cntp));
      STAMP(4);
    } else if constexpr (n == QOFF + 6) {
#pragma unroll
      for (int i = 0; i <= n - WIN; ++i) o = mfma(msk & P[i], V[i], o);
    } else {
      o = mfma(sel, V[n - WIN], o);
      asm volatile("" : "+v"(o));
      if constexpr (n == 14) {
#pragma unroll 1
        for (u32 spin = 0; spin < SPIN_CAP * 8; ++spin) {
          const u32 f = *(volatile LAS u32*)(L + L_F + a * 8 + (lane >> 5) * 4);
          F = __uint_as_float(f);
          if (none(f == SENT)) break;
          __builtin_amdgcn_s_sleep(1);
        }
      }
    }
    SB();
  });
  *(LAS f32x4*)(L + L_O + a * 1024 + lane * 16) = o * F;
  u32 arr;
  asm volatile("s_mov_b64 exec, 1\n\tds_add_rtn_u32 %0, %1, %2\n\ts_mov_b64 exec, -1\n\ts_waitcnt lgkmcnt(0)"
               : "=&v"(arr) : "v"(cntp), "v"(1u) : "memory");
  arr = __builtin_amdgcn_readfirstlane(arr);
  STAMP(5);
  if (arr == (br ? 6u : 0u)) {
    f32x4 t;
    if (br) {
      f32x4 u[8];
#pragma unroll
      for (int k = 0; k < 8; ++k) u[k] = *(LAS f32x4*)(L + L_O + k * 1024 + lane * 16);
      t = ((u[0] + u[1]) + (u[2] + u[3])) + ((u[4] + u[5]) + (u[6] + u[7]));
    } else {
      t = *(LAS f32x4*)(L + L_O + 8 * 1024 + lane * 16) + *(LAS f32x4*)(L + L_O + 9 * 1024 + lane * 16);
    }
    __builtin_amdgcn_raw_buffer_store_b128(u32x4{tw(t[0], tag), tw(t[1], tag), tw(t[2], tag), tw(t[3], tag)}, rs, ooff, 0, SC1);
    STAMP(8);
  }
  if (a == 9) FINISH();
  sfor<16 + WOPRE, 20>([&](auto kc) { constexpr int k = decltype(kc)::v; until<slot_ns(20 + k)>(t0); ISSUE(k); });
  SB();
  BARRIER();
  const u32x4 e = *(LAS u32x4*)(L + L_OF + (lane >> 3) * 16);
  const f32x2 y = gemv_tile(e, msk, V + 16);
  __builtin_amdgcn_raw_buffer_store_b64(u32x2{tw(y[0], tag), tw(y[1], tag)}, rs, yo, 0, SC1);
  STAMP(7);
  FINISH();
}

extern "C" void kernel_launch(void* const* d_in, const int* in_sizes, int n_in, void* d_out, int out_size, void* d_ws,
                              size_t ws_size, hipStream_t stream) {
  typedef const unsigned char* P;
  fused_decode<<<256, NTHR, 0, stream>>>((P)d_in[0], (P)d_in[1], (P)d_in[2], (P)d_in[3], (P)d_in[4], (P)d_in[5], (P)d_in[6],
                                         (P)d_in[7], (P)d_in[8], (P)d_in[9], (unsigned char*)d_out, (unsigned char*)d_ws);
}
```

```cpp
#define EARLYPF 2
#define EPO 1
#define CODEB 10200
#define DEFA 300
#define OUTPRE 1
#define WOH 30
#define WOPRE 1
#define PWO 260
#define GWO 50
#define D1 1350
#define D2 1350
#define OAUX 0
#define PACE 266
#define GAB 20
#define GBA 10
#define TK 1250
#define TQ 4450
#define DS 13
#define DW 0
#define DK 0
#define DM 5
#define DV 0
#include <hip/hip_runtime.h>
#include <hip/hip_bf16.h>
#include <stdint.h>

#define STAMPS 0
#define WIN 5
#define STAG 33
#define QSLEEP 16
#define QN 0
#define DBG 0
#define LEAD 0
#define TOUCH 0
#define WOMAP 0
#define W3TH -1
#define RED 0
#define LATEPF 0
#define MEAS 0
#define OPK 0
#define WOPH 0
#define WOST STAG
#define D1A D1
#define D2A D2
#define TLEAD 1300
#define BMAP 0
#define GF 0
#define GN 3
#define MLS 35
#define PAUX SC1
#define NOPUB 0
#define SLIP 0
#define SLTH 3
#define SL0 3
#define SL1 36
#define STR 0
#define STF 1
#define STN 8
#define GAK GAB
#define AUXO 2

typedef unsigned int u32;
typedef unsigned long long u64;
typedef u32 u32x2 __attribute__((ext_vector_type(2)));
typedef u32 u32x4 __attribute__((ext_vector_type(4)));
typedef float f32x2 __attribute__((ext_vector_type(2)));
typedef float f32x4 __attribute__((ext_vector_type(4)));
typedef __bf16 bf16x2 __attribute__((ext_vector_type(2)));
typedef __bf16 bf16x8 __attribute__((ext_vector_type(8)));
#define GAS __attribute__((address_space(1)))
#define LAS __attribute__((address_space(3)))
typedef __amdgpu_buffer_rsrc_t rsrc_t;

constexpr int NWAVE = 10, NTHR = NWAVE * 64;
constexpr u32 SENT = 0xFFFFFFFFu;
constexpr u32 SPIN_CAP = 1u << 17;
constexpr int WS_EPOCH = 0;
constexpr int WS_Q = 4096;
constexpr int WS_ML = WS_Q + 256 * 512;
constexpr int WS_O = WS_ML + 256 * 128;
constexpr int WS_Y = WS_O + 256 * 2048;
constexpr int WS_END = WS_Y + 256 * 2304;
constexpr int WS_STAMP = 4 << 20;
constexpr int L_QP = 0;
constexpr int L_ML = 2304;
constexpr int L_F = L_ML + 256;
constexpr int L_FLAG = L_F + 128;
constexpr int L_OF = 2944;
constexpr int L_Q = 3072;
constexpr int L_T0 = 5120;
constexpr int L_O = 6144;
constexpr int L_BYTES = L_O + 16 * 1024;
constexpr int SC1 = 16, NT = 2;
constexpr u32 CODE_BYTES = CODEB;

__device__ __forceinline__ rsrc_t mkrs(const void* p, u32 bytes) { return __builtin_amdgcn_make_buffer_rsrc((void*)p, 0, bytes, 0x00020000); }
__device__ __forceinline__ u32x4 ldnt(rsrc_t r, u32 voff, u32 soff) { return __builtin_amdgcn_raw_buffer_load_b128(r, voff, soff, NT); }
__device__ __forceinline__ u32x4 ldsc(rsrc_t r, u32 voff, u32 soff) { return __builtin_amdgcn_raw_buffer_load_b128(r, voff, soff, SC1); }
__device__ __forceinline__ u32 badtag(u32x4 v, u32 tag) { return (v[1] ^ tag) | (v[3] ^ tag); }
__device__ __forceinline__ u32 tw(u32 x, u32 tag) { return __builtin_amdgcn_perm(x, tag, 0x07060500u); }
__device__ __forceinline__ u32 or4(u32x4 v) { return (v[0] | v[1]) | (v[2] | v[3]); }
__device__ __forceinline__ f32x4 mfma(u32x4 a, u32x4 b, f32x4 c) {
  return __builtin_amdgcn_mfma_f32_16x16x32_bf16(__builtin_bit_cast(bf16x8, a), __builtin_bit_cast(bf16x8, b), c, 0, 0, 0);
}
__device__ __forceinline__ u32 cvtpk(float lo, float hi) {
  bf16x2 r = {(__bf16)lo, (__bf16)hi};
  return __builtin_bit_cast(u32, r);
}
__device__ __forceinline__ float asf(u32 x) { return __uint_as_float(x); }
__device__ __forceinline__ u32 asu(float x) { return __float_as_uint(x); }
__device__ __forceinline__ float rbf(float x) { return asf(cvtpk(x, x) & 0xffff0000u); }
__device__ __forceinline__ f32x4 asf4(u32x4 x) { return __builtin_bit_cast(f32x4, x); }
template <int C> __device__ __forceinline__ float dpp(float x) {
  return __builtin_bit_cast(float, __builtin_amdgcn_mov_dpp(__builtin_bit_cast(int, x), C, 0xf, 0xf, true));
}
template <int C> __device__ __forceinline__ int dppi(int x) { return __builtin_amdgcn_mov_dpp(x, C, 0xf, 0xf, true); }
template <int FROM, int TO> __device__ __forceinline__ u32 bankmov(u32 old, u32 src) {
  constexpr int n = ((TO - FROM) & 3) * 4;
  return __builtin_amdgcn_update_dpp(old, src, n ? 0x120 + n : 0xE4, 0xf, 1 << TO, false);
}
constexpr int XOR1 = 0xB1, XOR2 = 0x4E, HMIR = 0x141, ROR8 = 0x128, ROR4 = 0x124;
__device__ __forceinline__ float allmax4(float x) {
  x = fmaxf(x, dpp<ROR4>(x)); x = fmaxf(x, dpp<ROR8>(x));
  auto s = __builtin_amdgcn_permlane16_swap(asu(x), asu(x), false, false);
  x = fmaxf(asf(s[0]), asf(s[1]));
  auto t = __builtin_amdgcn_permlane32_swap(asu(x), asu(x), false, false);
  return fmaxf(asf(t[0]), asf(t[1]));
}
__device__ __forceinline__ float allsum4(float x) {
  x += dpp<ROR8>(x); x += dpp<ROR4>(x);
  auto s = __builtin_amdgcn_permlane16_swap(asu(x), asu(x), false, false);
  x = asf(s[0]) + asf(s[1]);
  auto t = __builtin_amdgcn_permlane32_swap(asu(x), asu(x), false, false);
  return asf(t[0]) + asf(t[1]);
}
__device__ __forceinline__ bool none(bool p) { return __builtin_amdgcn_ballot_w64(p) == 0; }
__device__ __forceinline__ f32x2 gemv_tile(u32x4 e, u32x4 msk, const u32x4* w) {
  f32x4 y = {0.f, 0.f, 0.f, 0.f};
#pragma unroll
  for (int i = 0; i < 4; ++i) { y = mfma(msk & e[i], w[i], y); __builtin_amdgcn_sched_barrier(0); }
  auto t0 = __builtin_amdgcn_permlane32_swap(asu(y[0]), asu(y[2]), false, false);
  auto t1 = __builtin_amdgcn_permlane32_swap(asu(y[1]), asu(y[3]), false, false);
  return f32x2{asf(t0[0]) + dpp<ROR8>(asf(t0[1])), asf(t1[0]) + dpp<ROR8>(asf(t1[1]))};
}
__device__ __forceinline__ u32x4 opack(f32x4 t, u32 tag) {
  auto p = __builtin_amdgcn_permlane16_swap(asu(t[0]), asu(t[2]), false, false);
  auto q = __builtin_amdgcn_permlane16_swap(asu(t[1]), asu(t[3]), false, false);
  return u32x4{tw(p[0], tag), tw(q[0], tag), tw(p[1], tag), tw(q[1], tag)};
}
template <int I> struct IC { static constexpr int v = I; };
template <int LO, int HI, class Fn> __device__ __forceinline__ void sfor(Fn&& f) {
  if constexpr (LO < HI) { f(IC<LO>{}); sfor<LO + 1, HI>(f); }
}
constexpr int issued_after(int n) { return n < 0 ? 0 : (n + 1 < 20 + WOPRE ? n + 1 : 20 + WOPRE); }
constexpr int ramp(int g, int from) { return g < from ? 0 : (g - from + 1 < GN ? g - from + 1 : GN); }
constexpr int slot_ns(int g) {
  return TK + (g <= 40 ? g * PACE : 40 * PACE + (g - 40) * PWO) + GAK * ramp(g, 4 + GF) + GAB * ramp(g, 24 + GF) + GBA * ramp(g, 20 + GF) + (g >= 40 ? GWO : 0);
}
#define SB() __builtin_amdgcn_sched_barrier(0)
#define VMWAIT(n) asm volatile("s_waitcnt vmcnt(%0)" ::"n"(n) : "memory")
#define LGKWAIT() asm volatile("s_waitcnt lgkmcnt(0)" ::: "memory")
#define BARRIER() asm volatile("s_waitcnt lgkmcnt(0)\n\ts_barrier" ::: "memory")
template <int NS> __device__ __forceinline__ void until(u32 t0) {
  asm volatile("1:\n\ts_memrealtime vcc\n\ts_waitcnt lgkmcnt(0)\n\ts_sub_u32 vcc_lo, vcc_lo, %0\n\ts_cmp_lt_i32 vcc_lo, %1\n\ts_cbranch_scc1 1b"
               : : "s"(t0), "n"(NS / 10) : "memory", "scc", "vcc");
}
#define RT(x) asm volatile("s_memrealtime %0\n\ts_waitcnt lgkmcnt(0)" : "=s"(x)::"memory")
#define STAMP(i) do { if ((i) == DS && wave == DW) { if (DV) VMWAIT(0); RT(tdiag); } } while (0)
#define STAMPV(i, v) do {} while (0)
#define FINISH() do { GAS u64* sp_ = (GAS u64*)((GAS unsigned char*)ws_ + (6 << 20)); \
  if (wave == DW && lane == 0) { __hip_atomic_store(sp_ + b, tstart, __ATOMIC_RELAXED, __HIP_MEMORY_SCOPE_AGENT); __hip_atomic_store(sp_ + 256 + b, tdiag, __ATOMIC_RELAXED, __HIP_MEMORY_SCOPE_AGENT); } \
  if (wave == 0 && b == 0) { u64 t; do { __builtin_amdgcn_s_sleep(4); RT(t); } while (t < tstart + 3500); \
    u64 mn = ~0ull, mx = 0, dn = ~0ull, sum = 0; for (int i = 0; i < 4; ++i) { u64 v = __hip_atomic_load(sp_ + i * 64 + lane, __ATOMIC_RELAXED, __HIP_MEMORY_SCOPE_AGENT); u64 d = __hip_atomic_load(sp_ + 256 + i * 64 + lane, __ATOMIC_RELAXED, __HIP_MEMORY_SCOPE_AGENT); mn = v < mn ? v : mn; mx = d > mx ? d : mx; dn = d < dn ? d : dn; sum += d; } \
    for (int m_ = 1; m_ < 64; m_ <<= 1) { u64 o1 = __shfl_xor(mn, m_), o2 = __shfl_xor(mx, m_), o3 = __shfl_xor(dn, m_), o4 = __shfl_xor(sum, m_); mn = o1 < mn ? o1 : mn; mx = o2 > mx ? o2 : mx; dn = o3 < dn ? o3 : dn; sum += o4; } \
    u64 xv_ = DK == 0 ? mx - mn : DK == 1 ? dn - mn : sum / 256 - mn; if (xv_ > 4000) xv_ = 4000; \
    do { __builtin_amdgcn_s_sleep(4); RT(t); } while (t < mn + 5000 + DM * xv_); } __builtin_amdgcn_endpgm(); } while (0)

__global__ void __launch_bounds__(NTHR, 3) __attribute__((aligned(4096)))
fused_decode(const unsigned char* xA, const unsigned char* kA, const unsigned char* vA, const unsigned char* wqA,
             const unsigned char* woA, const unsigned char* xB, const unsigned char* kB, const unsigned char* vB,
             const unsigned char* wqB, const unsigned char* woB, unsigned char* out_, unsigned char* ws_) {
  __shared__ __attribute__((aligned(16))) unsigned char smem[L_BYTES];
  LAS unsigned char* L = (LAS unsigned char*)smem;
  u64 tstart, tdiag;
  asm volatile("s_memrealtime %0" : "=s"(tstart));
  const int b = blockIdx.x, lane = threadIdx.x & 63;
  const int wave = __builtin_amdgcn_readfirstlane(threadIdx.x >> 6);
  const int x = b & 7, j = b >> 3;
  const int a = wave - 1;
  const int rbr = x >> 2, rh = (x >> 1) & 1, cb = (x & 1) * 32 + j;
  const int row = rh * 288 + a * 32 + 4 * (lane >> 3);
  const u32 qoff = (u32)row * 8192 + cb * 128 + (lane & 7) * 16;
  const u32 nq = wave ? 576 * 8192 : 0, nx = wave ? 1152 : 0;
  asm volatile("" ::"v"(qoff), "v"(row * 2), "s"(nq), "s"(nx));
  SB();
  asm volatile("" ::"s"(xA), "s"(kA), "s"(vA), "s"(wqA), "s"(woA), "s"(xB), "s"(kB), "s"(vB), "s"(wqB), "s"(woB), "s"(out_), "s"(ws_));
  u32x4 W[4];
  const rsrc_t rq = mkrs(rbr ? wqB : wqA, nq);
#pragma unroll
  for (int i = 0; i < 4; ++i) W[i] = ldnt(rq, qoff, i * 8192);
  const u32x2 xv = __builtin_amdgcn_raw_buffer_load_b64(mkrs(rbr ? xB : xA, nx), row * 2, 0, 0);
  SB();
  u32 t0 = (u32)__builtin_amdgcn_s_memrealtime();
  u64 pc;
  asm volatile("s_getpc_b64 %0" : "=s"(pc));
  asm volatile("s_waitcnt lgkmcnt(0)" : "+s"(tstart)); tdiag = tstart;
  const rsrc_t rcode = mkrs((const void*)(pc & ~(u64)255), (wave == 0 && (EARLYPF == 1 || j == 0)) ? CODE_BYTES : 0);
  u32 c0 = 0, c1 = 0;
  if (wave == 0) { c0 = __builtin_amdgcn_raw_buffer_load_b32(rcode, lane * 128, 0, 0); c1 = __builtin_amdgcn_raw_buffer_load_b32(rcode, lane * 128, 8192, 0); }
  *(LAS u32x2*)(L + threadIdx.x * 8) = u32x2{SENT, SENT};
  if (wave == 0) *(LAS u32*)(L + L_T0) = t0;
  BARRIER();
#define HEAD_OF(x_, k_) ((((x_) & 1) ^ 1) * 8 + ((x_) >> 1) * 2 + (k_))
  const int h = HEAD_OF(x, j >> 4), s = j & 15;
  const int xs = x ^ (1 + 3 * (j >> 4));
  const int hc = HEAD_OF(xs, (j >> 3) & 1), rc = j & 7;
  const rsrc_t rs = mkrs(ws_, WS_END);
  const int ooff = WS_O + (h * 16 + ((lane >> 4) & 1) * 4 + ((lane & 15) >> 2)) * 2048 + s * 128 + (lane & 3) * 32 + (lane >> 5) * 16;
  if (wave == 0) {
    const u32 tag = __builtin_amdgcn_raw_buffer_load_b32(rs, WS_EPOCH, 0, SC1) + 1u;
    asm volatile("" ::"v"(c0), "v"(c1));
    *(LAS u32*)(L + L_FLAG + 4) = tag;
    u32 tagb = tag & 0xffu;
    STAMP(1);
    {
      u32 t[9];
#pragma unroll 1
      for (u32 spin = 0; spin < SPIN_CAP * 8; ++spin) {
#pragma unroll
        for (int k = 0; k < 9; ++k) t[k] = *(volatile LAS u32*)(L + L_QP + k * 256 + lane * 4);
        if (none(max(max(max(max(t[0], t[1]), max(t[2], t[3])), max(max(t[4], t[5]), max(t[6], t[7]))), t[8]) == SENT)) break;
        __builtin_amdgcn_s_sleep(1);
      }
      const float qp = ((asf(t[0]) + asf(t[1])) + (asf(t[2]) + asf(t[3]))) + ((asf(t[4]) + asf(t[5])) + (asf(t[6]) + asf(t[7]))) + asf(t[8]);
      __builtin_amdgcn_raw_buffer_store_b64(u32x2{asu(qp), (tag & 0xffffu) | (t0 << 16)}, rs, WS_Q + (x * 32 + j) * 512 + lane * 8, 0, SC1);
    }
    STAMP(2);
    {
      u32x4 v[8];
      u32x2 tv;
      u32 spin = 0;
      until<TQ>(s ? t0 : t0 - LEAD / 10);
#pragma unroll 1
      for (; spin < SPIN_CAP; ++spin) {
        asm volatile("" ::: "memory");
#pragma unroll
        for (int i = 0; i < 8; ++i) v[i] = ldsc(rs, WS_Q + h * 2048 + lane * 16 + ((i >> 1) & 1) * 1024, ((i >> 2) * 2 + (i & 1)) * 32768);
        tv = __builtin_amdgcn_raw_buffer_load_b64(rs, (lane & 7) == x ? WS_END : WS_Q + (lane & 7) * 16384 + 31 * 512, 0, SC1);
        if ((lane & 7) == x) tv[1] = (tag & 0xffffu) | (t0 << 16);
        u32 bad = tv[1] ^ tag;
#pragma unroll
        for (int i = 0; i < 8; ++i) bad |= badtag(v[i], tag);
        if (none((bad & 0xffffu) != 0)) break;
        __builtin_amdgcn_s_sleep(QSLEEP);
      }
      STAMPV(22, spin + 1);
#pragma unroll
      for (int i = 0; i < 4; ++i)
        *(LAS u32*)(L + L_Q + i * 256 + lane * 4) = cvtpk(asf(v[2 * i][0]) + asf(v[2 * i + 1][0]), asf(v[2 * i][2]) + asf(v[2 * i + 1][2]));
      int d = (int)(tv[1] - (t0 << 16)) >> 16;
      d = max(d, dppi<XOR1>(d)); d = max(d, dppi<XOR2>(d)); d = max(d, dppi<HMIR>(d));
      d = __builtin_amdgcn_readfirstlane(d);
      t0 += d > 300 ? 300 : d;
      *(LAS u32*)(L + L_T0 + 4) = WOH >= 0 ? t0 - (d < WOH ? d : WOH) + rbr * (WOPH / 10) : (d <= W3TH ? t0 : t0 - 1000);
      *(LAS u32*)(L + L_T0 + 8) = d > 300 ? 300 : d;
      STAMPV(16, t0);
      LGKWAIT();
      *(volatile LAS u32*)(L + L_FLAG) = 0;
    }
    STAMP(4);
    {
      u32x2 ml;
#pragma unroll 1
      for (u32 spin = 0; spin < SPIN_CAP * 8; ++spin) {
        ml = *(volatile LAS u32x2*)(L + L_ML + (lane & 31) * 8);
        if (none(max(ml[0], ml[1]) == SENT)) break;
        __builtin_amdgcn_s_sleep(2);
      }
      STAMP(6);
      const float m = asf(ml[0]);
      float M = fmaxf(m, dpp<XOR2>(m)); M = fmaxf(M, dpp<ROR4>(M)); M = fmaxf(M, dpp<ROR8>(M));
      const float F = __builtin_amdgcn_exp2f(m - M);
      *(LAS float*)(L + L_F + (lane & 31) * 4) = F;
      float l = asf(ml[1]) * F;
      l += dpp<XOR2>(l); l += dpp<ROR8>(l); l += dpp<ROR4>(l);
      __builtin_amdgcn_raw_buffer_store_b128(u32x4{asu(M), tag, asu(l), tag}, rs,
                                             (lane & 14) == 0 ? WS_ML + (h * 16 + s) * 128 + ((lane >> 4) * 2 + (lane & 1)) * 16 : WS_END, 0, SC1);
    }
    u32x4 oA;
    {
#pragma unroll 1
      for (u32 spin = 0; spin < SPIN_CAP * 8; ++spin) {
        if (*(volatile LAS u32*)(L + L_FLAG + 8) == 7u) break;
        __builtin_amdgcn_s_sleep(2);
      }
      asm volatile("" ::: "memory");
      f32x4 t = {0.f, 0.f, 0.f, 0.f};
#pragma unroll
      for (int k = 0; k < 8; ++k) {
        float f0 = *(LAS float*)(L + L_F + k * 8), f1 = *(LAS float*)(L + L_F + k * 8 + 4);
        asm("" : "+v"(f0)); asm("" : "+v"(f1));
        t += *(LAS f32x4*)(L + L_O + k * 1024 + lane * 16) * f32x4{f0, f0, f1, f1};
      }
      oA = opack(t, tag);
      if (!DEFA) __builtin_amdgcn_raw_buffer_store_b128(oA, rs, ooff, 0, SC1);
    }
    const int moff = WS_ML + (hc * 16 + (lane & 15)) * 128 + (rbr * 2 + (rc >> 2)) * 16;
    const int ioff = WS_O + (hc * 16 + rbr * 8 + rc) * 2048 + lane * 16;
    const int br2 = rbr ^ 1;
    const int rr = RED == 0 ? ((j >> 2) * 4 + (x & 3)) * 4 + (j & 3) : j * 4 + (x & 3);
    constexpr int NRED = 72, NY = 4;
    const int yoff = WS_Y + (br2 * 128 + (lane >> 1)) * 2304 + rr * 32 + (lane & 1) * 16;
    const int outoff = (lane & 14) == 0 ? (br2 * 576 + rr * 8 + 4 * (lane & 1) + (lane >> 4)) * 2 : 4096;
    const rsrc_t rout = mkrs(out_, 2304);
    STAMP(7);
    until<slot_ns(MLS)>(rc ? t0 : t0 - LEAD / 10);
    float w0, w1;
    {
      u32x4 gv;
#pragma unroll 1
      for (u32 spin = 0; spin < SPIN_CAP; ++spin) {
        asm volatile("" ::: "memory");
        gv = ldsc(rs, moff, 0);
        if (none(badtag(gv, tag) != 0)) break;
      }
      const float gm = asf(gv[0]);
      float GM = fmaxf(gm, dpp<XOR1>(gm)); GM = fmaxf(GM, dpp<XOR2>(GM)); GM = fmaxf(GM, dpp<HMIR>(GM)); GM = fmaxf(GM, dpp<ROR8>(GM));
      const float ex = __builtin_amdgcn_exp2f(gm - GM);
      float GL = asf(gv[2]) * ex;
      GL += dpp<XOR1>(GL); GL += dpp<XOR2>(GL); GL += dpp<HMIR>(GL); GL += dpp<ROR8>(GL);
      const float w = ex / GL;
      w0 = __shfl(w, lane >> 3); w1 = __shfl(w, 8 + (lane >> 3));
    }
    LAS unsigned char* ofp = L + L_OF + (lane & 7) * 16 + (lane >> 4) * 4;
    asm volatile("" : "+v"(ofp), "+v"(tagb) : "v"(w0), "v"(w1), "v"(yoff), "v"(ioff), "v"(outoff), "s"(rout));
    STAMP(8);
    if (rr < NRED) __builtin_amdgcn_raw_buffer_store_b16((unsigned short)0, rout, outoff, 0, SC1);
    if (DEFA) { until<slot_ns(39) + DEFA>(t0); __builtin_amdgcn_raw_buffer_store_b128(oA, rs, ooff, 0, PAUX); }
    if (rbr || D1A == D1) {
#pragma unroll 1
      for (u32 spin = 0; spin < SPIN_CAP * 8; ++spin) {
        if (*(volatile LAS u32*)(L + L_FLAG + 12) == 7u) break;
        __builtin_amdgcn_s_sleep(1);
      }
    }
    until<slot_ns(39) + D1>(rbr ? t0 : t0 - (D1 - D1A) / 10);
    STAMP(9);
    {
      u32x4 v0, v1;
      u32 spin = 0;
#pragma unroll 1
      for (;;) {
        v0 = ldsc(rs, ioff, 0) ^ tagb;
        v1 = ldsc(rs, ioff, 1024) ^ tagb;
        if (none(((or4(v0) | or4(v1)) & 0xffu) != 0)) break;
        asm volatile("s_add_u32 %0, %0, 1" : "+s"(spin)::"memory", "scc");
        if (spin >= SPIN_CAP) break;
      }
      STAMP(10); STAMPV(20, spin + 1);
      const f32x4 ov = asf4(v0) * w0 + asf4(v1) * w1;
      auto p0 = __builtin_amdgcn_permlane32_swap(asu(ov[0]), asu(ov[2]), false, false);
      auto p1 = __builtin_amdgcn_permlane32_swap(asu(ov[1]), asu(ov[3]), false, false);
      auto p2 = __builtin_amdgcn_permlane16_swap(asu(asf(p0[0]) + asf(p0[1])), asu(asf(p1[0]) + asf(p1[1])), false, false);
      float z = asf(p2[0]) + asf(p2[1]);
      z += dpp<ROR8>(z);
      *(LAS u32*)ofp = cvtpk(z, z);
    }
    BARRIER();
    STAMP(11);
    if (rr < NRED) {
      until<slot_ns(39) + D1 + D2>(rbr ? t0 - (D2 - D2A) / 10 : t0);
      f32x4 y;
      u32 spin = 0;
#pragma unroll 1
      for (;;) {
        u32x4 v[NY];
#pragma unroll
        for (int i = 0; i < NY; ++i) v[i] = ldsc(rs, yoff, i * (128 / NY) * 2304);
        SB();
        u32 bad = 0;
#pragma unroll
        for (int i = 0; i < NY; ++i) {
          u32x4 t = v[i] ^ tagb;
          asm("" : "+v"(t));
          bad = (bad | t[0] | t[1]) | t[2] | t[3];
          y = i ? y + asf4(t) : asf4(t);
          asm volatile("" : "+v"(y), "+v"(bad));
        }
        if (none((bad & 0xffu) != 0)) break;
        asm volatile("s_add_u32 %0, %0, 1" : "+s"(spin)::"memory", "scc");
        if (spin >= SPIN_CAP) break;
      }
      STAMP(12); STAMPV(21, spin + 1);
      auto t0_ = __builtin_amdgcn_permlane32_swap(asu(y[0]), asu(y[2]), false, false);
      auto t1_ = __builtin_amdgcn_permlane32_swap(asu(y[1]), asu(y[3]), false, false);
      const float z0 = asf(t0_[0]) + asf(t0_[1]), z1 = asf(t1_[0]) + asf(t1_[1]);
      auto t2_ = __builtin_amdgcn_permlane16_swap(asu(z0), asu(z1), false, false);
      float z = asf(t2_[0]) + asf(t2_[1]);
      z += dpp<ROR8>(z); z += dpp<ROR4>(z);
      if constexpr (RED < 2) z += dpp<XOR2>(z);
      __builtin_amdgcn_raw_buffer_store_b16((unsigned short)cvtpk(z, z), rout, outoff, 0, OAUX);
      if (!EPO && b == 0) __builtin_amdgcn_raw_buffer_store_b32(tag, rs, lane == 0 ? WS_EPOCH : WS_END, 0, SC1);
    } else if (EPO && b == 255) {
#pragma unroll 1
      for (u32 spin = 0; spin < SPIN_CAP; ++spin) {
        asm volatile("" ::: "memory");
        u32 bad = 0;
#pragma unroll
        for (int i = 0; i < 4; ++i) bad |= __builtin_amdgcn_raw_buffer_load_b32(rs, WS_ML + lane * 128 + 4, i * 8192, SC1) ^ tag;
        if (none(bad != 0)) break;
      }
      __builtin_amdgcn_raw_buffer_store_b32(tag, rs, lane == 0 ? WS_EPOCH : WS_END, 0, SC1);
    }
    STAMP(13);
    FINISH();
  }
  t0 = __builtin_amdgcn_readfirstlane(*(volatile LAS u32*)(L + L_T0)) + (u32)(a * STAG) / 10u;
  const u32 t00 = t0;
  u32x4 msk;
#pragma unroll
  for (int k = 0; k < 4; ++k) msk[k] = ((lane & 7) >> 1) == k ? ((lane & 1) ? 0xffff0000u : 0x0000ffffu) : 0u;
  const int colp = (lane & 7) * 8 + ((lane >> 4) & 1) * 4 + (lane >> 5) * 2;
  auto qproj = [&]() {
    const u32x4 e = {__builtin_amdgcn_perm(xv[0], xv[0], 0x01000100u), __builtin_amdgcn_perm(xv[0], xv[0], 0x03020302u),
                     __builtin_amdgcn_perm(xv[1], xv[1], 0x01000100u), __builtin_amdgcn_perm(xv[1], xv[1], 0x03020302u)};
    const f32x2 qp = gemv_tile(e, msk, W);
    if (!(lane & 8)) *(LAS f32x2*)(L + L_QP + a * 256 + colp * 4) = qp;
  };
  const rsrc_t ro = mkrs((rbr ? woB : woA) + (u32)(hc * 256 + rc * 32) * 1152, 32 * 1152);
  const u32 wooff = (u32)(4 * (lane >> 3)) * 1152 + (lane & 7) * 16 + a * 128;
#define WOS(i) 0
#define WOV(i) ((i) * 1152)
  const int yo = (lane & 8) ? WS_END : WS_Y + (rbr * 128 + hc * 8 + rc) * 2304 + (a * 64 + colp) * 4;
#define WOORG() do { if (WOH >= 0) { const u32 tw_ = __builtin_amdgcn_readfirstlane(*(volatile LAS u32*)(L + L_T0 + 4)) + (u32)(a * WOST) / 10u; t0 = (int)(tw_ - t0) > 0 ? tw_ : t0; } } while (0)
#define SLIPCHK(g_) do { if constexpr (SLIP != 0 && (g_) >= SL0 && (g_) < SL1) t0 += __builtin_amdgcn_s_getreg(7 | (3 << 11)) >= SLTH ? SLIP : 0; } while (0)
#define ISSUE_WO(i) W[i] = __builtin_amdgcn_raw_buffer_load_b128(ro, wooff + WOV(i), WOS(i), AUXO)
  u32 tag;
  if (a == 8) {
    qproj();
#pragma unroll 1
    for (u32 spin = 0; spin < SPIN_CAP * 8; ++spin) { if (*(volatile LAS u32*)(L + L_FLAG) == 0) break; __builtin_amdgcn_s_sleep(8); }
    tag = *(volatile LAS u32*)(L + L_FLAG + 4);
    WOORG();
    sfor<0, WOPRE>([&](auto ic) { constexpr int i = decltype(ic)::v; until<slot_ns(40 + i)>(t0); ISSUE_WO(i); });
  } else {
    const u32 kvo = (u32)(h * 2048 + a * 16 + (s ^ ((a & 1) * 8))) * 1024;
    const u32 koff = (u32)((lane >> 2) & 3) * 256 + (lane & 3) * 64 + (lane >> 4) * 16, voff = lane * 16;
    constexpr u32 KVB = (15u << 17) + 1024;
    const int hb = h, jb = 0;
#define CHUNK(c_) ((c_) < 4 ? (u32)(12 + ((c_) ^ (h & 3))) << 17 : (u32)(((c_) - 4 + jb) ^ hb) << 17)
    u32x4 K[20], V[20];
    {
      const rsrc_t rkA = mkrs(kA + kvo, KVB), rkB = mkrs(kB + kvo, KVB);
      SB();
      sfor<0, 20>([&](auto cc) {
        constexpr int c = decltype(cc)::v;
        until<slot_ns(c)>(t0);
        SLIPCHK(c);
        VMWAIT(WIN - 1);
        K[c] = ldnt(c < 4 ? rkA : rkB, koff, CHUNK(c));
        SB();
        if constexpr (c == 3) { qproj(); SB(); STAMP(1); }
      });
    }
    const rsrc_t rvA = mkrs(vA + kvo, KVB), rvB = mkrs(vB + kvo, KVB);
    float S[5];
    u32 P[5];
    u32x4 QA, QB, sel = {0u, 0u, 0u, 0u};
    float F0, F1;
    u32 lead = 0;
    const u32 half = (lane & 1) ? 0xffff0000u : 0x0000ffffu;
    const float LOG2E = 1.4426950408889634f;
    f32x4 o = {0.f, 0.f, 0.f, 0.f};
    LAS unsigned char* op = L + L_O + a * 1024 + lane * 16;
    LAS unsigned char* cntB = L + L_FLAG + 12;
    u32 one = 1u;
    auto softmax = [&](auto loc, auto hic) {
      constexpr int lo = decltype(loc)::v, hi = decltype(hic)::v;
      float m = S[lo];
#pragma unroll
      for (int g = lo + 1; g < hi; ++g) m = fmaxf(m, S[g]);
      m = allmax4(m);
      float l = 0.f;
#pragma unroll
      for (int g = lo; g < hi; ++g) { const float p = __builtin_amdgcn_exp2f((S[g] - m) * LOG2E); l += p; P[g] = cvtpk(p, p) & half; }
      l = allsum4(l);
      if ((lane & 61) == 0) *(LAS f32x2*)(L + L_ML + (lo ? 128 : 0) + a * 16 + lane * 4) = f32x2{m * LOG2E, l};
    };
    sfor<0, 20 + WIN>([&](auto nc) {
      constexpr int n = decltype(nc)::v, c = n - WIN;
      static_assert(WIN >= QN + 2 && WIN + 4 >= QN + 6, "softmax before P.V");
      if constexpr (c >= 0) {
        sel[0] = bankmov<c & 3, 0>(sel[0], P[c >> 2]); sel[1] = bankmov<c & 3, 1>(sel[1], P[c >> 2]);
        sel[2] = bankmov<c & 3, 2>(sel[2], P[c >> 2]); sel[3] = bankmov<c & 3, 3>(sel[3], P[c >> 2]);
        asm volatile("" : "+v"(sel));
      }
      SB();
      if constexpr (STR != 0 && n >= STF && n < STF + STN) t0 = t00 + ((lead * (n - STF + 1)) >> 6);
      if constexpr (n == 20) WOORG();
      if constexpr (n < 20 + WOPRE) until<slot_ns(20 + n)>(t0);
      SLIPCHK(20 + n);
      if constexpr (n == 19) STAMPV(23, t0 - t00 + 1);
      VMWAIT(issued_after(n - 1) - (c + 1));
      if constexpr (n < 20) V[n] = ldnt(n < 4 ? rvA : rvB, voff, CHUNK(n));
      else if constexpr (n < 20 + WOPRE) ISSUE_WO(n - 20);
      SB();
      if constexpr (c >= 0) {
        if constexpr (c == 4) o = mfma(sel, V[c], f32x4{0.f, 0.f, 0.f, 0.f});
        else o = mfma(sel, V[c], o);
        asm volatile("" : "+v"(o));
        if constexpr (c == 3) {
          if (DBG == 2) asm volatile("s_nop 7\n\ts_nop 7" : "+v"(o));
          *(LAS f32x4*)op = o;
          if (DBG == 1) asm volatile("s_waitcnt lgkmcnt(0)" ::"v"(o) : "memory");
          asm volatile("s_mov_b64 exec, 1\n\tds_add_u32 %0, %1\n\ts_mov_b64 exec, -1" ::"v"(L + L_FLAG + 8), "v"(1u) : "memory");
        }
      }
      if constexpr (n == QN) {
        STAMP(2);
#pragma unroll 1
        for (u32 spin = 0; spin < SPIN_CAP * 8; ++spin) { if (*(volatile LAS u32*)(L + L_FLAG) == 0) break; __builtin_amdgcn_s_sleep(1); }
        asm volatile("" ::: "memory");
        STAMP(3);
        LAS unsigned char* qp = L + L_Q + ((lane >> 1) & 1) * 256 + ((lane >> 2) & 3) * 64 + (lane >> 4) * 16;
        QA = *(LAS u32x4*)qp;
        QB = *(LAS u32x4*)(qp + 512);
        tag = *(LAS u32*)(L + L_FLAG + 4);
        if (STR) lead = __builtin_amdgcn_readfirstlane(*(LAS u32*)(L + L_T0 + 8)) * STR;
      }
      if constexpr (n >= QN && n < QN + 5) {
        constexpr int g = n - QN;
        u32 T[4] = {0u, 0u, 0u, 0u};
        sfor<0, 4>([&](auto ic) {
          constexpr int i = decltype(ic)::v;
          const f32x4 d = mfma(K[g * 4 + i], g ? QB : QA, f32x4{0.f, 0.f, 0.f, 0.f});
          T[0] = bankmov<0, i>(T[0], asu(d[0])); T[1] = bankmov<1, i>(T[1], asu(d[1]));
          T[2] = bankmov<2, i>(T[2], asu(d[2])); T[3] = bankmov<3, i>(T[3], asu(d[3]));
        });
        S[g] = rbf(rbf((asf(T[0]) + asf(T[1])) + (asf(T[2]) + asf(T[3]))) * 0.08838834764831845f);
      }
      if constexpr (n == QN + 1) softmax(IC<0>{}, IC<1>{});
      if constexpr (n == QN + 5) { softmax(IC<1>{}, IC<5>{}); STAMP(4); }
      if constexpr (n == 18) {
#pragma unroll 1
        for (u32 spin = 0; spin < SPIN_CAP * 8; ++spin) {
          const u32x2 f = *(volatile LAS u32x2*)(L + L_F + 64 + a * 8);
          F0 = asf(f[0]); F1 = asf(f[1]);
          if (max(f[0], f[1]) != SENT) break;
          __builtin_amdgcn_s_sleep(1);
        }
        asm volatile("" : "+v"(F0), "+v"(F1), "+v"(cntB), "+v"(one) : "v"(yo), "v"(ooff), "v"(msk), "v"(wooff));
      }
      SB();
    });
    *(LAS f32x4*)(op + 8192) = o * f32x4{F0, F0, F1, F1};
    u32 arr;
    asm volatile("s_mov_b64 exec, 1\n\tds_add_rtn_u32 %0, %1, %2\n\ts_mov_b64 exec, -1" : "=&v"(arr) : "v"(cntB), "v"(one) : "memory");
    f32x4 u[8];
#pragma unroll
    for (int k = 0; k < 8; ++k) u[k] = *(LAS f32x4*)(L + L_O + 8192 + k * 1024 + lane * 16);
    SB();
    asm volatile("s_waitcnt lgkmcnt(8)" : "+v"(arr)::"memory");
    SB();
    if (__builtin_amdgcn_readfirstlane(arr) == 6u) {
      const f32x4 t = ((u[0] + u[1]) + (u[2] + u[3])) + ((u[4] + u[5]) + (u[6] + u[7]));
      if (NOPUB) until<slot_ns(39) + NOPUB>(__builtin_amdgcn_readfirstlane(*(volatile LAS u32*)(L + L_T0 + 4)) + 1000);
      __builtin_amdgcn_raw_buffer_store_b128(opack(t, tag), rs, ooff, 8 * 2048, PAUX);
      STAMP(8);
    } else {
#pragma unroll
      for (int k = 0; k < 8; ++k) asm volatile("" ::"v"(u[k]));
    }
    STAMP(5);
  }
  sfor<WOPRE, 4>([&](auto ic) {
    constexpr int i = decltype(ic)::v;
    until<slot_ns(40 + i)>(t0);
    if constexpr (i == 3 && W3TH >= 0) until<slot_ns(39) + D1 + 30>(__builtin_amdgcn_readfirstlane(*(volatile LAS u32*)(L + L_T0 + 4)));
    ISSUE_WO(i);
  });
  SB();
  LAS unsigned char* ep = L + L_OF + (lane >> 3) * 16;
  asm volatile("" : "+v"(ep));
  BARRIER();
  const u32x4 e = *(LAS u32x4*)ep;
  const f32x2 y = gemv_tile(e, msk, W);
  __builtin_amdgcn_raw_buffer_store_b64(u32x2{tw(asu(y[0]), tag), tw(asu(y[1]), tag)}, rs, yo, 0, SC1);
  STAMP(7);
  FINISH();
}

extern "C" void kernel_launch(void* const* d_in, const int* in_sizes, int n_in, void* d_out, int out_size, void* d_ws,
                              size_t ws_size, hipStream_t stream) {
  typedef const unsigned char* P;
  fused_decode<<<256, NTHR, 0, stream>>>((P)d_in[0], (P)d_in[1], (P)d_in[2], (P)d_in[3], (P)d_in[4], (P)d_in[5], (P)d_in[6],
                                         (P)d_in[7], (P)d_in[8], (P)d_in[9], (unsigned char*)d_out, (unsigned char*)d_ws);
}
```

```cpp
#define CODEB 11300
#define PRS 9
#define PRD 1650
#define G1 3
#define G2 4
#define G3 5
#define DS 13
#define DW 0
#define DK 0
#define DM 5
#define DV 0
#include <hip/hip_runtime.h>
#include <hip/hip_bf16.h>
#include <stdint.h>

#define STAMPS 0
#define WIN 6
#define PACE 266
#define TK 1300
#define PWO 270
#define WOPRE 2
#define STAG 33
#define WOH 30
#define TQ 4500
#define GK 30
#define PRW 34
#define GN0 2
#define PRT 3
#define GW 5
#define GT1 10
#define GT2 15
#define FORCE -1
#define WOE 0
#define PWE 310
#define QPS 3
#define DEFA 300
#define D1 1600
#define D2 1050
#define OAUX 0
#define MEAS 0

typedef unsigned int u32;
typedef unsigned long long u64;
typedef u32 u32x2 __attribute__((ext_vector_type(2)));
typedef u32 u32x4 __attribute__((ext_vector_type(4)));
typedef float f32x2 __attribute__((ext_vector_type(2)));
typedef float f32x4 __attribute__((ext_vector_type(4)));
typedef __bf16 bf16x2 __attribute__((ext_vector_type(2)));
typedef __bf16 bf16x8 __attribute__((ext_vector_type(8)));
#define GAS __attribute__((address_space(1)))
#define LAS __attribute__((address_space(3)))
typedef __amdgpu_buffer_rsrc_t rsrc_t;

constexpr int NWAVE = 10, NTHR = NWAVE * 64;
constexpr u32 SENT = 0xFFFFFFFFu;
constexpr u32 SPIN_CAP = 1u << 17;
constexpr int WS_EPOCH = 0;
constexpr int WS_R = 2048;
constexpr int WS_Q = 8192;
constexpr int WS_ML = WS_Q + 256 * 512;
constexpr int WS_O = WS_ML + 256 * 128;
constexpr int WS_Y = WS_O + 256 * 2048;
constexpr int WS_END = WS_Y + 256 * 2304;
constexpr int WS_STAMP = 4 << 20;
constexpr int L_QP = 0;
constexpr int L_ML = 2304;
constexpr int L_F = L_ML + 256;
constexpr int L_FLAG = L_F + 128;
constexpr int L_SCH = L_FLAG + 16;
constexpr int L_OF = 2944;
constexpr int L_Q = 3072;
constexpr int L_T0 = 5120;
constexpr int L_O = 6144;
constexpr int L_BYTES = L_O + 16 * 1024;
constexpr int SC1 = 16, NT = 2;
constexpr u32 CODE_BYTES = CODEB;

__device__ __forceinline__ rsrc_t mkrs(const void* p, u32 bytes) { return __builtin_amdgcn_make_buffer_rsrc((void*)p, 0, bytes, 0x00020000); }
__device__ __forceinline__ u32x4 ldnt(rsrc_t r, u32 voff, u32 soff) { return __builtin_amdgcn_raw_buffer_load_b128(r, voff, soff, NT); }
__device__ __forceinline__ u32x4 ldsc(rsrc_t r, u32 voff, u32 soff) { return __builtin_amdgcn_raw_buffer_load_b128(r, voff, soff, SC1); }
__device__ __forceinline__ u32 badtag(u32x4 v, u32 tag) { return (v[1] ^ tag) | (v[3] ^ tag); }
__device__ __forceinline__ u32 tw(u32 x, u32 tag) { return __builtin_amdgcn_perm(x, tag, 0x07060500u); }
__device__ __forceinline__ u32 or4(u32x4 v) { return (v[0] | v[1]) | (v[2] | v[3]); }
__device__ __forceinline__ f32x4 mfma(u32x4 a, u32x4 b, f32x4 c) {
  return __builtin_amdgcn_mfma_f32_16x16x32_bf16(__builtin_bit_cast(bf16x8, a), __builtin_bit_cast(bf16x8, b), c, 0, 0, 0);
}
__device__ __forceinline__ u32 cvtpk(float lo, float hi) {
  bf16x2 r = {(__bf16)lo, (__bf16)hi};
  return __builtin_bit_cast(u32, r);
}
__device__ __forceinline__ float asf(u32 x) { return __uint_as_float(x); }
__device__ __forceinline__ u32 asu(float x) { return __float_as_uint(x); }
__device__ __forceinline__ float rbf(float x) { return asf(cvtpk(x, x) & 0xffff0000u); }
__device__ __forceinline__ f32x4 asf4(u32x4 x) { return __builtin_bit_cast(f32x4, x); }
template <int C> __device__ __forceinline__ float dpp(float x) {
  return __builtin_bit_cast(float, __builtin_amdgcn_mov_dpp(__builtin_bit_cast(int, x), C, 0xf, 0xf, true));
}
template <int C> __device__ __forceinline__ int dppi(int x) { return __builtin_amdgcn_mov_dpp(x, C, 0xf, 0xf, true); }
template <int FROM, int TO> __device__ __forceinline__ u32 bankmov(u32 old, u32 src) {
  constexpr int n = ((TO - FROM) & 3) * 4;
  return __builtin_amdgcn_update_dpp(old, src, n ? 0x120 + n : 0xE4, 0xf, 1 << TO, false);
}
constexpr int XOR1 = 0xB1, XOR2 = 0x4E, HMIR = 0x141, ROR8 = 0x128, ROR4 = 0x124;
__device__ __forceinline__ float allmax4(float x) {
  x = fmaxf(x, dpp<ROR4>(x)); x = fmaxf(x, dpp<ROR8>(x));
  auto s = __builtin_amdgcn_permlane16_swap(asu(x), asu(x), false, false);
  x = fmaxf(asf(s[0]), asf(s[1]));
  auto t = __builtin_amdgcn_permlane32_swap(asu(x), asu(x), false, false);
  return fmaxf(asf(t[0]), asf(t[1]));
}
__device__ __forceinline__ float allsum4(float x) {
  x += dpp<ROR8>(x); x += dpp<ROR4>(x);
  auto s = __builtin_amdgcn_permlane16_swap(asu(x), asu(x), false, false);
  x = asf(s[0]) + asf(s[1]);
  auto t = __builtin_amdgcn_permlane32_swap(asu(x), asu(x), false, false);
  return asf(t[0]) + asf(t[1]);
}
__device__ __forceinline__ float fold4(f32x4 v) {
  auto p0 = __builtin_amdgcn_permlane32_swap(asu(v[0]), asu(v[2]), false, false);
  auto p1 = __builtin_amdgcn_permlane32_swap(asu(v[1]), asu(v[3]), false, false);
  auto p2 = __builtin_amdgcn_permlane16_swap(asu(asf(p0[0]) + asf(p0[1])), asu(asf(p1[0]) + asf(p1[1])), false, false);
  return asf(p2[0]) + asf(p2[1]);
}
__device__ __forceinline__ bool none(bool p) { return __builtin_amdgcn_ballot_w64(p) == 0; }
__device__ __forceinline__ f32x2 gemv_tile(u32x4 e, u32x4 msk, const u32x4* w) {
  f32x4 y = {0.f, 0.f, 0.f, 0.f};
#pragma unroll
  for (int i = 0; i < 4; ++i) { y = mfma(msk & e[i], w[i], y); __builtin_amdgcn_sched_barrier(0); }
  auto t0 = __builtin_amdgcn_permlane32_swap(asu(y[0]), asu(y[2]), false, false);
  auto t1 = __builtin_amdgcn_permlane32_swap(asu(y[1]), asu(y[3]), false, false);
  return f32x2{asf(t0[0]) + dpp<ROR8>(asf(t0[1])), asf(t1[0]) + dpp<ROR8>(asf(t1[1]))};
}
__device__ __forceinline__ u32x4 opack(f32x4 t, u32 tag) {
  auto p = __builtin_amdgcn_permlane16_swap(asu(t[0]), asu(t[2]), false, false);
  auto q = __builtin_amdgcn_permlane16_swap(asu(t[1]), asu(t[3]), false, false);
  return u32x4{tw(p[0], tag), tw(q[0], tag), tw(p[1], tag), tw(q[1], tag)};
}
template <int I> struct IC { static constexpr int v = I; };
template <int LO, int HI, class Fn> __device__ __forceinline__ void sfor(Fn&& f) {
  if constexpr (LO < HI) { f(IC<LO>{}); sfor<LO + 1, HI>(f); }
}
constexpr int issued_after(int n) { return n < 0 ? 0 : (n + 1 < 20 + WOPRE ? n + 1 : 20 + WOPRE); }
constexpr int ramp3(int k) { return k < 0 ? 0 : (k < 3 ? k : 3); }
constexpr int slot_ns(int g) {
  return TK + WOE * PWE + (g <= 40 ? g * PACE : 40 * PACE + (g - 40) * PWO) + GK * ramp3(g - 3);
}
#define SB() __builtin_amdgcn_sched_barrier(0)
#define VMWAIT(n) asm volatile("s_waitcnt vmcnt(%0)" ::"n"(n) : "memory")
#define LGKWAIT() asm volatile("s_waitcnt lgkmcnt(0)" ::: "memory")
#define BARRIER() asm volatile("s_waitcnt lgkmcnt(0)\n\ts_barrier" ::: "memory")
#define SPIN_INC(s_) asm volatile("s_add_u32 %0, %0, 1" : "+s"(s_)::"memory", "scc")
template <int NS> __device__ __forceinline__ void until(u32 t0) {
  asm volatile("1:\n\ts_memrealtime vcc\n\ts_waitcnt lgkmcnt(0)\n\ts_sub_u32 vcc_lo, vcc_lo, %0\n\ts_cmp_lt_i32 vcc_lo, %1\n\ts_cbranch_scc1 1b"
               : : "s"(t0), "n"(NS / 10) : "memory", "scc", "vcc");
}
struct Probe { u32 n, d; };
__device__ __forceinline__ Probe probe(const unsigned char* p) {
  Probe r;
  asm volatile("s_load_dword %0, %2, 0x0\n\ts_load_dword %0, %2, 0x11300\n\ts_load_dword %0, %2, 0x22600\n\ts_load_dword %0, %2, 0x33900\n\t"
               "s_load_dword %0, %2, 0x44c00\n\ts_load_dword %0, %2, 0x55f00\n\ts_load_dword %0, %2, 0x66200\n\ts_load_dword %0, %2, 0x77500\n\t"
               "s_load_dword %0, %2, 0x19800\n\ts_load_dword %0, %2, 0x2ab00\n\ts_load_dword %0, %2, 0x3be00\n\ts_load_dword %0, %2, 0x4c100\n\t"
               "s_sleep %3\n\ts_getreg_b32 %1, hwreg(HW_REG_IB_STS, 8, 4)"
               : "=&s"(r.d), "=&s"(r.n) : "s"(p), "n"(PRW) : "memory");
  return r;
}
__device__ __forceinline__ void probe_end(const Probe& r) { asm volatile("s_waitcnt lgkmcnt(0)" ::"s"(r.d) : "memory"); }
template <int SLEEP> __device__ __forceinline__ void lds_wait(LAS unsigned char* p, u32 want) {
#pragma unroll 1
  for (u32 spin = 0; spin < SPIN_CAP * 8; ++spin) { if (*(volatile LAS u32*)p == want) break; __builtin_amdgcn_s_sleep(SLEEP); }
  asm volatile("" ::: "memory");
}
#define RT(x) asm volatile("s_memrealtime %0\n\ts_waitcnt lgkmcnt(0)" : "=s"(x)::"memory")
#define STAMP(i) do { if ((i) == DS && wave == DW) { if (DV) VMWAIT(0); RT(tdiag); } } while (0)
#define STAMPV(i, v) do {} while (0)
#define FINISH() do { GAS u64* sp_ = (GAS u64*)((GAS unsigned char*)ws_ + (6 << 20)); \
  if (wave == DW && lane == 0) { __hip_atomic_store(sp_ + b, tstart, __ATOMIC_RELAXED, __HIP_MEMORY_SCOPE_AGENT); __hip_atomic_store(sp_ + 256 + b, tdiag, __ATOMIC_RELAXED, __HIP_MEMORY_SCOPE_AGENT); } \
  if (wave == 0 && b == 0) { u64 t; do { __builtin_amdgcn_s_sleep(4); RT(t); } while (t < tstart + 3500); \
    u64 mn = ~0ull, mx = 0, dn = ~0ull, sum = 0; for (int i = 0; i < 4; ++i) { u64 v = __hip_atomic_load(sp_ + i * 64 + lane, __ATOMIC_RELAXED, __HIP_MEMORY_SCOPE_AGENT); u64 d = __hip_atomic_load(sp_ + 256 + i * 64 + lane, __ATOMIC_RELAXED, __HIP_MEMORY_SCOPE_AGENT); mn = v < mn ? v : mn; mx = d > mx ? d : mx; dn = d < dn ? d : dn; sum += d; } \
    for (int m_ = 1; m_ < 64; m_ <<= 1) { u64 o1 = __shfl_xor(mn, m_), o2 = __shfl_xor(mx, m_), o3 = __shfl_xor(dn, m_), o4 = __shfl_xor(sum, m_); mn = o1 < mn ? o1 : mn; mx = o2 > mx ? o2 : mx; dn = o3 < dn ? o3 : dn; sum += o4; } \
    u64 xv_ = DK == 0 ? mx - mn : DK == 1 ? dn - mn : sum / 256 - mn; if (xv_ > 4000) xv_ = 4000; \
    do { __builtin_amdgcn_s_sleep(4); RT(t); } while (t < mn + 5000 + DM * (xv_ - 1300) + 4000 * decv); } __builtin_amdgcn_endpgm(); } while (0)

__global__ void __launch_bounds__(NTHR, 3) __attribute__((aligned(4096)))
fused_decode(const unsigned char* xA, const unsigned char* kA, const unsigned char* vA, const unsigned char* wqA,
             const unsigned char* woA, const unsigned char* xB, const unsigned char* kB, const unsigned char* vB,
             const unsigned char* wqB, const unsigned char* woB, unsigned char* out_, unsigned char* ws_) {
  __shared__ __attribute__((aligned(16))) unsigned char smem[L_BYTES];
  LAS unsigned char* L = (LAS unsigned char*)smem;
  u64 tstart, tdiag; u32 decv = 0;
  asm volatile("s_memrealtime %0" : "=s"(tstart));
  const int b = blockIdx.x, lane = threadIdx.x & 63;
  const int wave = __builtin_amdgcn_readfirstlane(threadIdx.x >> 6);
  const int x = b & 7, j = b >> 3;
  const int a = wave - 1;
  const int rbr = x >> 2, rh = (x >> 1) & 1, cb = (x & 1) * 32 + j;
  const int row = rh * 288 + a * 32 + 4 * (lane >> 3);
  const u32 qoff = (u32)row * 8192 + cb * 128 + (lane & 7) * 16;
  const u32 nq = wave ? 576 * 8192 : 0, nx = wave ? 1152 : 0;
  asm volatile("" ::"v"(qoff), "v"(row * 2), "s"(nq), "s"(nx));
  SB();
  asm volatile("" ::"s"(xA), "s"(kA), "s"(vA), "s"(wqA), "s"(woA), "s"(xB), "s"(kB), "s"(vB), "s"(wqB), "s"(woB), "s"(out_), "s"(ws_));
  u32x4 W[4];
  const rsrc_t rq = mkrs(rbr ? wqB : wqA, nq);
#pragma unroll
  for (int i = 0; i < 4; ++i) W[i] = ldnt(rq, qoff, i * 8192);
  const u32x2 xv = __builtin_amdgcn_raw_buffer_load_b64(mkrs(rbr ? xB : xA, nx), row * 2, 0, 0);
  SB();
  u32 t0 = (u32)__builtin_amdgcn_s_memrealtime();
  u64 pc;
  asm volatile("s_getpc_b64 %0" : "=s"(pc));
  asm volatile("s_waitcnt lgkmcnt(0)" : "+s"(tstart)); tdiag = tstart;
  u32 c0 = 0, c1 = 0;
  if (wave == 0) {
    const rsrc_t rcode = mkrs((const void*)(pc & ~(u64)4095), j == 0 ? CODE_BYTES : 0);
    c0 = __builtin_amdgcn_raw_buffer_load_b32(rcode, lane * 128, 0, 0);
    c1 = __builtin_amdgcn_raw_buffer_load_b32(rcode, lane * 128, 8192, 0);
    *(LAS u32*)(L + L_T0) = t0;
  }
  *(LAS u32x2*)(L + threadIdx.x * 8) = u32x2{SENT, SENT};
  BARRIER();
#define HEAD_OF(x_, k_) ((((x_) & 1) ^ 1) * 8 + ((x_) >> 1) * 2 + (k_))
  const int h = HEAD_OF(x, j >> 4), s = j & 15;
  const int xs = x ^ (1 + 3 * (j >> 4));
  const int hc = HEAD_OF(xs, (j >> 3) & 1), rc = j & 7;
  const rsrc_t rs = mkrs(ws_, WS_END);
  const int ooff = WS_O + (h * 16 + ((lane >> 4) & 1) * 4 + ((lane & 15) >> 2)) * 2048 + s * 128 + (lane & 3) * 32 + (lane >> 5) * 16;
  if (wave == 0) {
    const u32 tag = __builtin_amdgcn_raw_buffer_load_b32(rs, WS_EPOCH, 0, SC1) + 1u;
    asm volatile("" ::"v"(c0), "v"(c1));
    u32 tagb = tag & 0xffu;
    STAMP(1);
    {
      u32 t[9];
#pragma unroll 1
      for (u32 spin = 0; spin < SPIN_CAP * 8; ++spin) {
#pragma unroll
        for (int k = 0; k < 9; ++k) t[k] = *(volatile LAS u32*)(L + L_QP + k * 256 + lane * 4);
        if (none(max(max(max(max(t[0], t[1]), max(t[2], t[3])), max(max(t[4], t[5]), max(t[6], t[7]))), t[8]) == SENT)) break;
        __builtin_amdgcn_s_sleep(1);
      }
      const float qp = ((asf(t[0]) + asf(t[1])) + (asf(t[2]) + asf(t[3]))) + ((asf(t[4]) + asf(t[5])) + (asf(t[6]) + asf(t[7]))) + asf(t[8]);
      __builtin_amdgcn_raw_buffer_store_b64(u32x2{asu(qp), (tag & 0xffffu) | (t0 << 16)}, rs, WS_Q + (x * 32 + j) * 512 + lane * 8, 0, SC1);
    }
    STAMP(2);
    u32 lead, t1, t2;
    {
      u32x4 v[8];
      u32 tv;
      u32 spin = 0;
      until<TQ>(t0);
#pragma unroll 1
      for (; spin < SPIN_CAP; ++spin) {
        asm volatile("" ::: "memory");
#pragma unroll
        for (int i = 0; i < 8; ++i) v[i] = ldsc(rs, WS_Q + h * 2048 + lane * 16 + ((i >> 1) & 1) * 1024, ((i >> 2) * 2 + (i & 1)) * 32768);
        tv = __builtin_amdgcn_raw_buffer_load_b32(rs, (lane & 7) == x ? WS_END : WS_Q + (lane & 7) * 16384 + 31 * 512 + 4, 0, SC1);
        if ((lane & 7) == x) tv = (tag & 0xffffu) | (t0 << 16);
        u32 bad = tv ^ tag;
#pragma unroll
        for (int i = 0; i < 8; ++i) bad |= badtag(v[i], tag);
        if (none((bad & 0xffffu) != 0)) break;
        __builtin_amdgcn_s_sleep(16);
      }
      STAMPV(22, spin + 1);
#pragma unroll
      for (int i = 0; i < 4; ++i)
        *(LAS u32*)(L + L_Q + i * 256 + lane * 4) = cvtpk(asf(v[2 * i][0]) + asf(v[2 * i + 1][0]), asf(v[2 * i][2]) + asf(v[2 * i + 1][2]));
      int d = (int)(tv - (t0 << 16)) >> 16;
      d = max(d, dppi<XOR1>(d)); d = max(d, dppi<XOR2>(d)); d = max(d, dppi<HMIR>(d));
      d = min(__builtin_amdgcn_readfirstlane(d), 300);
      t0 += d;
      lead = min(d, WOH);
      *(LAS u32*)(L + L_FLAG + 4) = tag;
      STAMPV(16, t0);
      LGKWAIT();
      *(volatile LAS u32*)(L + L_FLAG) = 0;
    }
    STAMP(4);
    {
      u32x2 rp;
      until<slot_ns(PRS) + PRD>(t0);
      u32 spin = 0;
#pragma unroll 1
      for (; spin < SPIN_CAP; ++spin) {
        asm volatile("" ::: "memory");
        rp = __builtin_amdgcn_raw_buffer_load_b64(rs, lane < 40 ? WS_R + lane * 128 : WS_END, 0, SC1);
        if (none(lane < 40 && rp[1] != tag)) break;
      }
      const u64 slow5 = __builtin_amdgcn_ballot_w64(rp[0] != 0);
      const u32 slow = (u32)slow5;
      const bool fK = FORCE >= 0 ? FORCE & 1 : __builtin_popcount((u32)(slow5 >> 32)) >= PRT;
      const bool fA = FORCE >= 0 ? FORCE & 1 : __builtin_popcount(slow & 0xffu) >= PRT;
      const bool fB = FORCE >= 0 ? FORCE & 1 : __builtin_popcount(slow & 0xff00u) >= PRT;
      const bool fW = FORCE >= 0 ? FORCE >> 1 : fB || (__builtin_popcount(slow & 0xff0000u) >= PRT && __builtin_popcount(slow >> 24) >= PRT);
      decv = (fA ? 1 : 0) + (fB ? 2 : 0) + (fW ? 4 : 0) + (fK ? 8 : 0);
      STAMPV(23, 1 + (fA ? 1 : 0) + (fB ? 10 : 0) + (fW ? 100 : 0) + (fK ? 1000 : 0) + 10000 * spin);
      const u32 e1 = (fA ? G2 : 0) + (fK ? G1 : 0), e2 = fA || fB ? G3 : 0;
      t0 = __builtin_amdgcn_readfirstlane(t0 + 4 * e1 + 3 * e2);
      *(LAS u32x2*)(L + L_SCH) = u32x2{e1 | e2 << 8 | (fW ? 1u << 16 : 0u), t0 - lead + (fW ? GW : 0)};
      t1 = __builtin_amdgcn_readfirstlane(t0 + (fW ? GT1 : 0)); t2 = __builtin_amdgcn_readfirstlane(t0 + (fW ? GT2 : 0));
    }
    STAMP(5);
    {
      u32x2 ml;
#pragma unroll 1
      for (u32 spin = 0; spin < SPIN_CAP * 8; ++spin) {
        ml = *(volatile LAS u32x2*)(L + L_ML + (lane & 31) * 8);
        if (none(max(ml[0], ml[1]) == SENT)) break;
        __builtin_amdgcn_s_sleep(2);
      }
      STAMP(6);
      const float m = asf(ml[0]);
      float M = fmaxf(m, dpp<XOR2>(m)); M = fmaxf(M, dpp<ROR4>(M)); M = fmaxf(M, dpp<ROR8>(M));
      const float F = __builtin_amdgcn_exp2f(m - M);
      *(LAS float*)(L + L_F + (lane & 31) * 4) = F;
      float l = asf(ml[1]) * F;
      l += dpp<XOR2>(l); l += dpp<ROR8>(l); l += dpp<ROR4>(l);
      __builtin_amdgcn_raw_buffer_store_b128(u32x4{asu(M), tag, asu(l), tag}, rs,
                                             (lane & 14) == 0 ? WS_ML + (h * 16 + s) * 128 + ((lane >> 4) * 2 + (lane & 1)) * 16 : WS_END, 0, SC1);
    }
    u32x4 oA;
    {
      lds_wait<2>(L + L_FLAG + 8, 7u);
      f32x4 t = {0.f, 0.f, 0.f, 0.f};
#pragma unroll
      for (int k = 0; k < 8; ++k) {
        float f0 = *(LAS float*)(L + L_F + k * 8), f1 = *(LAS float*)(L + L_F + k * 8 + 4);
        asm("" : "+v"(f0)); asm("" : "+v"(f1));
        t += *(LAS f32x4*)(L + L_O + k * 1024 + lane * 16) * f32x4{f0, f0, f1, f1};
      }
      oA = opack(t, tag);
    }
    const int moff = WS_ML + (hc * 16 + (lane & 15)) * 128 + (rbr * 2 + (rc >> 2)) * 16;
    const int ioff = WS_O + (hc * 16 + rbr * 8 + rc) * 2048 + lane * 16;
    const int br2 = rbr ^ 1, rr = ((j >> 2) * 4 + (x & 3)) * 4 + (j & 3);
    const int yoff = WS_Y + (br2 * 128 + (lane >> 1)) * 2304 + rr * 32 + (lane & 1) * 16;
    const int outoff = (lane & 14) == 0 ? (br2 * 576 + rr * 8 + 4 * (lane & 1) + (lane >> 4)) * 2 : 4096;
    const rsrc_t rout = mkrs(out_, rr < 72 ? 2304 : 0);
    STAMP(7);
    until<slot_ns(35)>(t0);
    float w0, w1;
    {
      u32x4 gv;
#pragma unroll 1
      for (u32 spin = 0; spin < SPIN_CAP; ++spin) {
        asm volatile("" ::: "memory");
        gv = ldsc(rs, moff, 0);
        if (none(badtag(gv, tag) != 0)) break;
      }
      const float gm = asf(gv[0]);
      float GM = fmaxf(gm, dpp<XOR1>(gm)); GM = fmaxf(GM, dpp<XOR2>(GM)); GM = fmaxf(GM, dpp<HMIR>(GM)); GM = fmaxf(GM, dpp<ROR8>(GM));
      const float ex = __builtin_amdgcn_exp2f(gm - GM);
      float GL = asf(gv[2]) * ex;
      GL += dpp<XOR1>(GL); GL += dpp<XOR2>(GL); GL += dpp<HMIR>(GL); GL += dpp<ROR8>(GL);
      const float w = ex / GL;
      w0 = __shfl(w, lane >> 3); w1 = __shfl(w, 8 + (lane >> 3));
    }
    LAS unsigned char* ofp = L + L_OF + (lane & 7) * 16 + (lane >> 4) * 4;
    asm volatile("" : "+v"(ofp), "+v"(tagb) : "v"(w0), "v"(w1), "v"(yoff), "v"(ioff), "v"(outoff), "s"(rout));
    STAMP(8);
    __builtin_amdgcn_raw_buffer_store_b16((unsigned short)0, rout, outoff, 0, SC1);
    until<slot_ns(39) + DEFA>(t0);
    __builtin_amdgcn_raw_buffer_store_b128(oA, rs, ooff, 0, SC1);
    lds_wait<1>(L + L_FLAG + 12, 7u);
    until<slot_ns(39) + D1>(t1);
    STAMP(9);
    {
      u32x4 v0, v1;
      u32 spin = 0;
#pragma unroll 1
      for (;;) {
        v0 = ldsc(rs, ioff, 0) ^ tagb;
        v1 = ldsc(rs, ioff, 1024) ^ tagb;
        if (none(((or4(v0) | or4(v1)) & 0xffu) != 0)) break;
        SPIN_INC(spin);
        if (spin >= SPIN_CAP) break;
      }
      STAMP(10); STAMPV(20, spin + 1);
      float z = fold4(asf4(v0) * w0 + asf4(v1) * w1);
      z += dpp<ROR8>(z);
      *(LAS u32*)ofp = cvtpk(z, z);
    }
    BARRIER();
    STAMP(11);
    if (rr < 72) {
      until<slot_ns(39) + D1 + D2>(t2);
      f32x4 y;
      u32 spin = 0;
#pragma unroll 1
      for (;;) {
        u32x4 v[4];
#pragma unroll
        for (int i = 0; i < 4; ++i) v[i] = ldsc(rs, yoff, i * 32 * 2304);
        SB();
        u32 bad = 0;
#pragma unroll
        for (int i = 0; i < 4; ++i) {
          u32x4 t = v[i] ^ tagb;
          asm("" : "+v"(t));
          bad = (bad | t[0] | t[1]) | t[2] | t[3];
          y = i ? y + asf4(t) : asf4(t);
          asm volatile("" : "+v"(y), "+v"(bad));
        }
        if (none((bad & 0xffu) != 0)) break;
        SPIN_INC(spin);
        if (spin >= SPIN_CAP) break;
      }
      STAMP(12); STAMPV(21, spin + 1);
      float z = fold4(y);
      z += dpp<ROR8>(z); z += dpp<ROR4>(z); z += dpp<XOR2>(z);
      __builtin_amdgcn_raw_buffer_store_b16((unsigned short)cvtpk(z, z), rout, outoff, 0, OAUX);
    } else if (b == 255) {
#pragma unroll 1
      for (u32 spin = 0; spin < SPIN_CAP; ++spin) {
        asm volatile("" ::: "memory");
        u32 bad = 0;
#pragma unroll
        for (int i = 0; i < 4; ++i) bad |= __builtin_amdgcn_raw_buffer_load_b32(rs, WS_ML + lane * 128 + 4, i * 8192, SC1) ^ tag;
        if (none(bad != 0)) break;
      }
      __builtin_amdgcn_raw_buffer_store_b32(tag, rs, lane == 0 ? WS_EPOCH : WS_END, 0, SC1);
    }
    STAMP(13);
    FINISH();
  }
  t0 = __builtin_amdgcn_readfirstlane(*(volatile LAS u32*)(L + L_T0));
  const u32 stg = (u32)(a * STAG) / 10u;
  u32x4 msk;
#pragma unroll
  for (int k = 0; k < 4; ++k) msk[k] = ((lane & 7) >> 1) == k ? ((lane & 1) ? 0xffff0000u : 0x0000ffffu) : 0u;
  const int colp = (lane & 7) * 8 + ((lane >> 4) & 1) * 4 + (lane >> 5) * 2;
  auto qproj = [&]() {
    const u32x4 e = {__builtin_amdgcn_perm(xv[0], xv[0], 0x01000100u), __builtin_amdgcn_perm(xv[0], xv[0], 0x03020302u),
                     __builtin_amdgcn_perm(xv[1], xv[1], 0x01000100u), __builtin_amdgcn_perm(xv[1], xv[1], 0x03020302u)};
    const f32x2 qp = gemv_tile(e, msk, W);
    if (!(lane & 8)) *(LAS f32x2*)(L + L_QP + a * 256 + colp * 4) = qp;
  };
  const rsrc_t ro = mkrs((rbr ? woB : woA) + (u32)(hc * 256 + rc * 32) * 1152, 32 * 1152);
  const u32 wooff = (u32)(4 * (lane >> 3)) * 1152 + (lane & 7) * 16 + a * 128;
  const int yo = (lane & 8) ? WS_END : WS_Y + (rbr * 128 + hc * 8 + rc) * 2304 + (a * 64 + colp) * 4;
  u32x4 WO[4];
#define ISSUE_WO(i) WO[i] = ldnt(ro, wooff + (i) * 1152, 0)
  sfor<0, WOE>([&](auto ic) { constexpr int i = decltype(ic)::v; until<TK + i * PWE>(t0 + stg); ISSUE_WO(i); SB(); });
  u32 tag, e1, e2, hold, two;
  auto getsch = [&](auto sc) {
    u32x2 v;
#pragma unroll 1
    for (u32 spin = 0; spin < SPIN_CAP * 8; ++spin) { v = *(volatile LAS u32x2*)(L + L_SCH); if (v[0] != SENT) break; __builtin_amdgcn_s_sleep(decltype(sc)::v); }
    asm volatile("" ::: "memory");
    const u32 e = __builtin_amdgcn_readfirstlane(v[0]);
    e1 = e & 0xffu; e2 = (e >> 8) & 0xffu; hold = (e >> 16) & 1u;
    two = __builtin_amdgcn_readfirstlane(v[1]) + stg;
  };
  if (a == 8) {
    tag = __builtin_amdgcn_raw_buffer_load_b32(rs, j >= 27 ? WS_EPOCH : WS_END, 0, SC1) + 1u;
    qproj();
    if (j >= 27) {
      const int roff = lane < 8 ? WS_R + ((31 - j) * 8 + x) * 128 + lane * 16 : WS_END;
      asm volatile("" ::"v"(tag), "v"(roff));
      const unsigned char* pp = (j == 27 ? kA : (j & 2) ? ((j & 1) ? vA : vB) : ((j & 1) ? woA : woB)) + (u32)x * 0x8f900;
      until<slot_ns(PRS)>(t0);
      const Probe pr = probe(pp);
      __builtin_amdgcn_raw_buffer_store_b128(u32x4{pr.n, tag, pr.n, tag}, rs, roff, 0, SC1);
      probe_end(pr);
      STAMPV(20, pr.n + 1);
      STAMP(9); VMWAIT(0); STAMP(10);
    }
    getsch(IC<8>{});
    tag = *(LAS u32*)(L + L_FLAG + 4);
    t0 = two;
    hold = 0;
    sfor<0, 2>([&](auto ic) { constexpr int i = decltype(ic)::v; until<slot_ns(40 + i)>(t0); ISSUE_WO(i); });
  } else {
    t0 += stg;
    const u32 tb = t0;
    const u32 kvo = (u32)(h * 2048 + a * 16 + (s ^ ((a & 1) * 8))) * 1024;
    const u32 koff = (u32)((lane >> 2) & 3) * 256 + (lane & 3) * 64 + (lane >> 4) * 16, voff = lane * 16;
    constexpr u32 KVB = (15u << 17) + 1024;
#define CHUNK(c_) ((c_) < 4 ? (u32)(12 + ((c_) ^ (h & 3))) << 17 : (u32)(((c_) - 4) ^ h) << 17)
    u32x4 K[20], V[20];
    {
      const rsrc_t rkA = mkrs(kA + kvo, KVB), rkB = mkrs(kB + kvo, KVB);
      SB();
      sfor<0, 20>([&](auto gc) {
        constexpr int g = decltype(gc)::v, c = g;
        until<slot_ns(g)>(t0);
        VMWAIT(WIN - 1);
        K[c] = ldnt(c < 4 ? rkA : rkB, koff, CHUNK(c));
        SB();
        if constexpr (g == QPS) { qproj(); SB(); STAMP(1); }
      });
    }
    const rsrc_t rvA = mkrs(vA + kvo, KVB), rvB = mkrs(vB + kvo, KVB);
    float S[5];
    u32 P[5];
    u32x4 QA, QB, sel = {0u, 0u, 0u, 0u};
    float F0, F1;
    const u32 half = (lane & 1) ? 0xffff0000u : 0x0000ffffu;
    const float LOG2E = 1.4426950408889634f;
    f32x4 o = {0.f, 0.f, 0.f, 0.f};
    LAS unsigned char* op = L + L_O + a * 1024 + lane * 16;
    LAS unsigned char* cntB = L + L_FLAG + 12;
    u32 one = 1u;
    auto softmax = [&](auto loc, auto hic) {
      constexpr int lo = decltype(loc)::v, hi = decltype(hic)::v;
      float m = S[lo];
#pragma unroll
      for (int g = lo + 1; g < hi; ++g) m = fmaxf(m, S[g]);
      m = allmax4(m);
      float l = 0.f;
#pragma unroll
      for (int g = lo; g < hi; ++g) { const float p = __builtin_amdgcn_exp2f((S[g] - m) * LOG2E); l += p; P[g] = cvtpk(p, p) & half; }
      l = allsum4(l);
      if ((lane & 61) == 0) *(LAS f32x2*)(L + L_ML + (lo ? 128 : 0) + a * 16 + lane * 4) = f32x2{m * LOG2E, l};
    };
    sfor<0, 20 + WIN>([&](auto nc) {
      constexpr int n = decltype(nc)::v, c = n - WIN;
      static_assert(WIN >= 2, "softmax before P.V");
      if constexpr (c >= 0) {
        sel[0] = bankmov<c & 3, 0>(sel[0], P[c >> 2]); sel[1] = bankmov<c & 3, 1>(sel[1], P[c >> 2]);
        sel[2] = bankmov<c & 3, 2>(sel[2], P[c >> 2]); sel[3] = bankmov<c & 3, 3>(sel[3], P[c >> 2]);
        asm volatile("" : "+v"(sel));
      }
      SB();
      if constexpr (n < GN0) {
        const u32 e = __builtin_amdgcn_readfirstlane(*(volatile LAS u32*)(L + L_SCH));
        e1 = e == SENT ? 0 : e & 0xffu;
      }
      if constexpr (n == GN0) getsch(IC<1>{});
      if constexpr (n < 4) t0 = tb + (n + 1) * e1;
      if constexpr (n >= 4 && n < 7) t0 += e2;
      if constexpr (n == 20) t0 = two;
      if constexpr (n < 21) until<slot_ns(20 + n)>(t0);
      if constexpr (n == 21) until<slot_ns(20 + n)>(t0 - hold * 1000);
      VMWAIT(issued_after(n - 1) - (c + 1));
      if constexpr (n < 20) V[n] = ldnt(n < 4 ? rvA : rvB, voff, CHUNK(n));
      else if constexpr (n == 20) ISSUE_WO(0);
      else if constexpr (n == 21) WO[1] = ldnt(ro, wooff + 1152, hold << 20);
      SB();
      if constexpr (c >= 0) {
        if constexpr (c == 4) o = mfma(sel, V[c], f32x4{0.f, 0.f, 0.f, 0.f});
        else o = mfma(sel, V[c], o);
        asm volatile("" : "+v"(o));
        if constexpr (c == 3) {
          *(LAS f32x4*)op = o;
          asm volatile("s_mov_b64 exec, 1\n\tds_add_u32 %0, %1\n\ts_mov_b64 exec, -1" ::"v"(L + L_FLAG + 8), "v"(1u) : "memory");
        }
      }
      if constexpr (n == 0) {
        STAMP(2);
        lds_wait<1>(L + L_FLAG, 0u);
        STAMP(3);
        LAS unsigned char* qp = L + L_Q + ((lane >> 1) & 1) * 256 + ((lane >> 2) & 3) * 64 + (lane >> 4) * 16;
        QA = *(LAS u32x4*)qp;
        QB = *(LAS u32x4*)(qp + 512);
        tag = *(LAS u32*)(L + L_FLAG + 4);
      }
      if constexpr (n < 5) {
        constexpr int g = n;
        u32 T[4] = {0u, 0u, 0u, 0u};
        sfor<0, 4>([&](auto ic) {
          constexpr int i = decltype(ic)::v;
          const f32x4 d = mfma(K[g * 4 + i], g ? QB : QA, f32x4{0.f, 0.f, 0.f, 0.f});
          T[0] = bankmov<0, i>(T[0], asu(d[0])); T[1] = bankmov<1, i>(T[1], asu(d[1]));
          T[2] = bankmov<2, i>(T[2], asu(d[2])); T[3] = bankmov<3, i>(T[3], asu(d[3]));
        });
        S[g] = rbf(rbf((asf(T[0]) + asf(T[1])) + (asf(T[2]) + asf(T[3]))) * 0.08838834764831845f);
      }
      if constexpr (n == 1) softmax(IC<0>{}, IC<1>{});
      if constexpr (n == 5) { softmax(IC<1>{}, IC<5>{}); STAMP(4); }
      if constexpr (n == 18) {
#pragma unroll 1
        for (u32 spin = 0; spin < SPIN_CAP * 8; ++spin) {
          const u32x2 f = *(volatile LAS u32x2*)(L + L_F + 64 + a * 8);
          F0 = asf(f[0]); F1 = asf(f[1]);
          if (max(f[0], f[1]) != SENT) break;
          __builtin_amdgcn_s_sleep(1);
        }
        asm volatile("" : "+v"(F0), "+v"(F1), "+v"(cntB), "+v"(one) : "v"(yo), "v"(ooff), "v"(msk), "v"(wooff));
      }
      SB();
    });
    *(LAS f32x4*)(op + 8192) = o * f32x4{F0, F0, F1, F1};
    u32 arr;
    asm volatile("s_mov_b64 exec, 1\n\tds_add_rtn_u32 %0, %1, %2\n\ts_mov_b64 exec, -1" : "=&v"(arr) : "v"(cntB), "v"(one) : "memory");
    f32x4 u[8];
#pragma unroll
    for (int k = 0; k < 8; ++k) u[k] = *(LAS f32x4*)(L + L_O + 8192 + k * 1024 + lane * 16);
    SB();
    asm volatile("s_waitcnt lgkmcnt(8)" : "+v"(arr)::"memory");
    SB();
    if (__builtin_amdgcn_readfirstlane(arr) == 6u) {
      const f32x4 t = ((u[0] + u[1]) + (u[2] + u[3])) + ((u[4] + u[5]) + (u[6] + u[7]));
      __builtin_amdgcn_raw_buffer_store_b128(opack(t, tag), rs, ooff, 8 * 2048, SC1);
      STAMP(8);
    } else {
#pragma unroll
      for (int k = 0; k < 8; ++k) asm volatile("" ::"v"(u[k]));
    }
    STAMP(5);
  }
  if (hold) { until<slot_ns(41)>(t0); ISSUE_WO(1); }
  sfor<WOPRE, 4 - WOE>([&](auto ic) { constexpr int i = decltype(ic)::v; until<slot_ns(40 + i)>(t0); ISSUE_WO(WOE + i); });
  SB();
  LAS unsigned char* ep = L + L_OF + (lane >> 3) * 16;
  asm volatile("" : "+v"(ep));
  BARRIER();
  const u32x4 e = *(LAS u32x4*)ep;
  const f32x2 y = gemv_tile(e, msk, WO);
  __builtin_amdgcn_raw_buffer_store_b64(u32x2{tw(asu(y[0]), tag), tw(asu(y[1]), tag)}, rs, yo, 0, SC1);
  STAMP(7);
  FINISH();
}

extern "C" void kernel_launch(void* const* d_in, const int* in_sizes, int n_in, void* d_out, int out_size, void* d_ws,
                              size_t ws_size, hipStream_t stream) {
  typedef const unsigned char* P;
  fused_decode<<<256, NTHR, 0, stream>>>((P)d_in[0], (P)d_in[1], (P)d_in[2], (P)d_in[3], (P)d_in[4], (P)d_in[5], (P)d_in[6],
                                         (P)d_in[7], (P)d_in[8], (P)d_in[9], (unsigned char*)d_out, (unsigned char*)d_ws);
}
```

```cpp
#define DEFA 300
#define EARLYPF 2
#define WOPRE 2
#define PWO 300
#define CODEB 9900
#define PACE 272
#define D1 1400
#define D2 1400
#include <hip/hip_runtime.h>
#include <hip/hip_bf16.h>
#include <stdint.h>

#define STAMPS 0
#define WIN 5
#define TK 1550
#define STAG 33
#define TQ 4800
#define QSLEEP 16
#define QN 0
#define DBG 0
#define LEAD 0
#define TOUCH 0
#define WOMAP 0
#define W3TH -1
#define RED 0
#define OAUX 16
#define AUXO 2

typedef unsigned int u32;
typedef unsigned long long u64;
typedef u32 u32x2 __attribute__((ext_vector_type(2)));
typedef u32 u32x4 __attribute__((ext_vector_type(4)));
typedef float f32x2 __attribute__((ext_vector_type(2)));
typedef float f32x4 __attribute__((ext_vector_type(4)));
typedef __bf16 bf16x2 __attribute__((ext_vector_type(2)));
typedef __bf16 bf16x8 __attribute__((ext_vector_type(8)));
#define GAS __attribute__((address_space(1)))
#define LAS __attribute__((address_space(3)))
typedef __amdgpu_buffer_rsrc_t rsrc_t;

constexpr int NWAVE = 10, NTHR = NWAVE * 64;
constexpr u32 SENT = 0xFFFFFFFFu;
constexpr u32 SPIN_CAP = 1u << 17;
constexpr int WS_EPOCH = 0;
constexpr int WS_Q = 4096;
constexpr int WS_ML = WS_Q + 256 * 512;
constexpr int WS_O = WS_ML + 256 * 128;
constexpr int WS_Y = WS_O + 256 * 2048;
constexpr int WS_END = WS_Y + 256 * 2304;
constexpr int WS_STAMP = 4 << 20;
constexpr int L_QP = 0;
constexpr int L_ML = 2304;
constexpr int L_F = L_ML + 256;
constexpr int L_FLAG = L_F + 128;
constexpr int L_OF = 2944;
constexpr int L_Q = 3072;
constexpr int L_T0 = 5120;
constexpr int L_O = 6144;
constexpr int L_BYTES = L_O + 16 * 1024;
constexpr int SC1 = 16, NT = 2;
constexpr u32 CODE_BYTES = CODEB;

__device__ __forceinline__ rsrc_t mkrs(const void* p, u32 bytes) { return __builtin_amdgcn_make_buffer_rsrc((void*)p, 0, bytes, 0x00020000); }
__device__ __forceinline__ u32x4 ldnt(rsrc_t r, u32 voff, u32 soff) { return __builtin_amdgcn_raw_buffer_load_b128(r, voff, soff, NT); }
__device__ __forceinline__ u32x4 ldsc(rsrc_t r, u32 voff, u32 soff) { return __builtin_amdgcn_raw_buffer_load_b128(r, voff, soff, SC1); }
__device__ __forceinline__ u32 badtag(u32x4 v, u32 tag) { return (v[1] ^ tag) | (v[3] ^ tag); }
__device__ __forceinline__ u32 tw(u32 x, u32 tag) { return __builtin_amdgcn_perm(x, tag, 0x07060500u); }
__device__ __forceinline__ u32 or4(u32x4 v) { return (v[0] | v[1]) | (v[2] | v[3]); }
__device__ __forceinline__ f32x4 mfma(u32x4 a, u32x4 b, f32x4 c) {
  return __builtin_amdgcn_mfma_f32_16x16x32_bf16(__builtin_bit_cast(bf16x8, a), __builtin_bit_cast(bf16x8, b), c, 0, 0, 0);
}
__device__ __forceinline__ u32 cvtpk(float lo, float hi) {
  bf16x2 r = {(__bf16)lo, (__bf16)hi};
  return __builtin_bit_cast(u32, r);
}
__device__ __forceinline__ float asf(u32 x) { return __uint_as_float(x); }
__device__ __forceinline__ u32 asu(float x) { return __float_as_uint(x); }
__device__ __forceinline__ float rbf(float x) { return asf(cvtpk(x, x) & 0xffff0000u); }
__device__ __forceinline__ f32x4 asf4(u32x4 x) { return __builtin_bit_cast(f32x4, x); }
template <int C> __device__ __forceinline__ float dpp(float x) {
  return __builtin_bit_cast(float, __builtin_amdgcn_mov_dpp(__builtin_bit_cast(int, x), C, 0xf, 0xf, true));
}
template <int C> __device__ __forceinline__ int dppi(int x) { return __builtin_amdgcn_mov_dpp(x, C, 0xf, 0xf, true); }
template <int FROM, int TO> __device__ __forceinline__ u32 bankmov(u32 old, u32 src) {
  constexpr int n = ((TO - FROM) & 3) * 4;
  return __builtin_amdgcn_update_dpp(old, src, n ? 0x120 + n : 0xE4, 0xf, 1 << TO, false);
}
constexpr int XOR1 = 0xB1, XOR2 = 0x4E, HMIR = 0x141, ROR8 = 0x128, ROR4 = 0x124;
__device__ __forceinline__ float allmax4(float x) {
  x = fmaxf(x, dpp<ROR4>(x)); x = fmaxf(x, dpp<ROR8>(x));
  auto s = __builtin_amdgcn_permlane16_swap(asu(x), asu(x), false, false);
  x = fmaxf(asf(s[0]), asf(s[1]));
  auto t = __builtin_amdgcn_permlane32_swap(asu(x), asu(x), false, false);
  return fmaxf(asf(t[0]), asf(t[1]));
}
__device__ __forceinline__ float allsum4(float x) {
  x += dpp<ROR8>(x); x += dpp<ROR4>(x);
  auto s = __builtin_amdgcn_permlane16_swap(asu(x), asu(x), false, false);
  x = asf(s[0]) + asf(s[1]);
  auto t = __builtin_amdgcn_permlane32_swap(asu(x), asu(x), false, false);
  return asf(t[0]) + asf(t[1]);
}
__device__ __forceinline__ bool none(bool p) { return __builtin_amdgcn_ballot_w64(p) == 0; }
__device__ __forceinline__ f32x2 gemv_tile(u32x4 e, u32x4 msk, const u32x4* w) {
  f32x4 y = {0.f, 0.f, 0.f, 0.f};
#pragma unroll
  for (int i = 0; i < 4; ++i) y = mfma(msk & e[i], w[i], y);
  auto t0 = __builtin_amdgcn_permlane32_swap(asu(y[0]), asu(y[2]), false, false);
  auto t1 = __builtin_amdgcn_permlane32_swap(asu(y[1]), asu(y[3]), false, false);
  return f32x2{asf(t0[0]) + dpp<ROR8>(asf(t0[1])), asf(t1[0]) + dpp<ROR8>(asf(t1[1]))};
}
__device__ __forceinline__ u32x4 opack(f32x4 t, u32 tag) {
  auto p = __builtin_amdgcn_permlane16_swap(asu(t[0]), asu(t[2]), false, false);
  auto q = __builtin_amdgcn_permlane16_swap(asu(t[1]), asu(t[3]), false, false);
  return u32x4{tw(p[0], tag), tw(q[0], tag), tw(p[1], tag), tw(q[1], tag)};
}
template <int I> struct IC { static constexpr int v = I; };
template <int LO, int HI, class Fn> __device__ __forceinline__ void sfor(Fn&& f) {
  if constexpr (LO < HI) { f(IC<LO>{}); sfor<LO + 1, HI>(f); }
}
constexpr int issued_after(int n) { return n < 0 ? 0 : (n + 1 < 20 + WOPRE ? n + 1 : 20 + WOPRE); }
constexpr int slot_ns(int g) { return TK + (g <= 40 ? g * PACE : 40 * PACE + (g - 40) * PWO); }
#define SB() __builtin_amdgcn_sched_barrier(0)
#define VMWAIT(n) asm volatile("s_waitcnt vmcnt(%0)" ::"n"(n) : "memory")
#define LGKWAIT() asm volatile("s_waitcnt lgkmcnt(0)" ::: "memory")
#define BARRIER() asm volatile("s_waitcnt lgkmcnt(0)\n\ts_barrier" ::: "memory")
template <int NS> __device__ __forceinline__ void until(u32 t0) {
  asm volatile("1:\n\ts_memrealtime vcc\n\ts_waitcnt lgkmcnt(0)\n\ts_sub_u32 vcc_lo, vcc_lo, %0\n\ts_cmp_lt_i32 vcc_lo, %1\n\ts_cbranch_scc1 1b"
               : : "s"(t0), "n"(NS / 10) : "memory", "scc", "vcc");
}
#define STAMP(i) do {} while (0)
#define STAMPV(i, v) do {} while (0)
#define FINISH() __builtin_amdgcn_endpgm()

__global__ void __launch_bounds__(NTHR, 3)
fused_decode(const unsigned char* xA, const unsigned char* kA, const unsigned char* vA, const unsigned char* wqA,
             const unsigned char* woA, const unsigned char* xB, const unsigned char* kB, const unsigned char* vB,
             const unsigned char* wqB, const unsigned char* woB, unsigned char* out_, unsigned char* ws_) {
  __shared__ __attribute__((aligned(16))) unsigned char smem[L_BYTES];
  LAS unsigned char* L = (LAS unsigned char*)smem;
  const int b = blockIdx.x, lane = threadIdx.x & 63;
  const int wave = __builtin_amdgcn_readfirstlane(threadIdx.x >> 6);
  const int x = b & 7, j = b >> 3;
  const int a = wave - 1;
  const int rbr = x >> 2, rh = (x >> 1) & 1, cb = (x & 1) * 32 + j;
  const int row = rh * 288 + a * 32 + 4 * (lane >> 3);
  const u32 qoff = (u32)row * 8192 + cb * 128 + (lane & 7) * 16;
  const u32 nq = wave ? 576 * 8192 : 0, nx = wave ? 1152 : 0;
  asm volatile("" ::"v"(qoff), "v"(row * 2), "s"(nq), "s"(nx));
  SB();
  asm volatile("" ::"s"(xA), "s"(kA), "s"(vA), "s"(wqA), "s"(woA), "s"(xB), "s"(kB), "s"(vB), "s"(wqB), "s"(woB), "s"(out_), "s"(ws_));
  u32x4 W[4];
  const rsrc_t rq = mkrs(rbr ? wqB : wqA, nq);
#pragma unroll
  for (int i = 0; i < 4; ++i) W[i] = ldnt(rq, qoff, i * 8192);
  const u32x2 xv = __builtin_amdgcn_raw_buffer_load_b64(mkrs(rbr ? xB : xA, nx), row * 2, 0, 0);
  SB();
  u32 t0 = (u32)__builtin_amdgcn_s_memrealtime();
  u64 pc;
  asm volatile("s_getpc_b64 %0" : "=s"(pc));
  const rsrc_t rcode = mkrs((const void*)(pc & ~(u64)255), (wave == 0 && (EARLYPF == 1 || j == 0)) ? CODE_BYTES : 0);
  u32 c0 = 0, c1 = 0;
  if (wave == 0) { c0 = __builtin_amdgcn_raw_buffer_load_b32(rcode, lane * 128, 0, 0); c1 = __builtin_amdgcn_raw_buffer_load_b32(rcode, lane * 128, 8192, 0); }
  *(LAS u32x2*)(L + threadIdx.x * 8) = u32x2{SENT, SENT};
  if (wave == 0) *(LAS u32*)(L + L_T0) = t0;
  BARRIER();
#define HEAD_OF(x_, k_) ((((x_) & 1) ^ 1) * 8 + ((x_) >> 1) * 2 + (k_))
  const int h = HEAD_OF(x, j >> 4), s = j & 15;
  const int xs = x ^ (1 + 3 * (j >> 4));
  const int hc = HEAD_OF(xs, (j >> 3) & 1), rc = j & 7;
  const rsrc_t rs = mkrs(ws_, WS_END);
  const int ooff = WS_O + (h * 16 + ((lane >> 4) & 1) * 4 + ((lane & 15) >> 2)) * 2048 + s * 128 + (lane & 3) * 32 + (lane >> 5) * 16;
  if (wave == 0) {
    const u32 tag = __builtin_amdgcn_raw_buffer_load_b32(rs, WS_EPOCH, 0, SC1) + 1u;
    asm volatile("" ::"v"(c0), "v"(c1));
    *(LAS u32*)(L + L_FLAG + 4) = tag;
    const u32 tagb = tag & 0xffu;
    STAMP(1);
    {
      u32 t[9];
#pragma unroll 1
      for (u32 spin = 0; spin < SPIN_CAP * 8; ++spin) {
#pragma unroll
        for (int k = 0; k < 9; ++k) t[k] = *(volatile LAS u32*)(L + L_QP + k * 256 + lane * 4);
        if (none(max(max(max(max(t[0], t[1]), max(t[2], t[3])), max(max(t[4], t[5]), max(t[6], t[7]))), t[8]) == SENT)) break;
        __builtin_amdgcn_s_sleep(1);
      }
      const float qp = ((asf(t[0]) + asf(t[1])) + (asf(t[2]) + asf(t[3]))) + ((asf(t[4]) + asf(t[5])) + (asf(t[6]) + asf(t[7]))) + asf(t[8]);
      __builtin_amdgcn_raw_buffer_store_b64(u32x2{asu(qp), (tag & 0xffffu) | (t0 << 16)}, rs, WS_Q + (x * 32 + j) * 512 + lane * 8, 0, SC1);
    }
    STAMP(2);
    {
      u32x4 v[8];
      u32x2 tv;
      u32 spin = 0;
      until<TQ>(s ? t0 : t0 - LEAD / 10);
#pragma unroll 1
      for (; spin < SPIN_CAP; ++spin) {
        asm volatile("" ::: "memory");
#pragma unroll
        for (int i = 0; i < 8; ++i) v[i] = ldsc(rs, WS_Q + h * 2048 + lane * 16 + ((i >> 1) & 1) * 1024, ((i >> 2) * 2 + (i & 1)) * 32768);
        tv = __builtin_amdgcn_raw_buffer_load_b64(rs, (lane & 7) == x ? WS_END : WS_Q + (lane & 7) * 16384 + 31 * 512, 0, SC1);
        if ((lane & 7) == x) tv[1] = (tag & 0xffffu) | (t0 << 16);
        u32 bad = tv[1] ^ tag;
#pragma unroll
        for (int i = 0; i < 8; ++i) bad |= badtag(v[i], tag);
        if (none((bad & 0xffffu) != 0)) break;
        __builtin_amdgcn_s_sleep(QSLEEP);
      }
      STAMPV(22, spin + 1);
#pragma unroll
      for (int i = 0; i < 4; ++i)
        *(LAS u32*)(L + L_Q + i * 256 + lane * 4) = cvtpk(asf(v[2 * i][0]) + asf(v[2 * i + 1][0]), asf(v[2 * i][2]) + asf(v[2 * i + 1][2]));
      int d = (int)(tv[1] - (t0 << 16)) >> 16;
      d = max(d, dppi<XOR1>(d)); d = max(d, dppi<XOR2>(d)); d = max(d, dppi<HMIR>(d));
      d = __builtin_amdgcn_readfirstlane(d);
      t0 += d > 300 ? 300 : d;
      *(LAS u32*)(L + L_T0 + 4) = d <= W3TH ? t0 : t0 - 1000;
      STAMPV(16, t0);
      LGKWAIT();
      *(volatile LAS u32*)(L + L_FLAG) = 0;
    }
    STAMP(4);
    {
      u32x2 ml;
#pragma unroll 1
      for (u32 spin = 0; spin < SPIN_CAP * 8; ++spin) {
        ml = *(volatile LAS u32x2*)(L + L_ML + (lane & 31) * 8);
        if (none(max(ml[0], ml[1]) == SENT)) break;
        __builtin_amdgcn_s_sleep(2);
      }
      STAMP(6);
      const float m = asf(ml[0]);
      float M = fmaxf(m, dpp<XOR2>(m)); M = fmaxf(M, dpp<ROR4>(M)); M = fmaxf(M, dpp<ROR8>(M));
      const float F = __builtin_amdgcn_exp2f(m - M);
      *(LAS float*)(L + L_F + (lane & 31) * 4) = F;
      float l = asf(ml[1]) * F;
      l += dpp<XOR2>(l); l += dpp<ROR8>(l); l += dpp<ROR4>(l);
      __builtin_amdgcn_raw_buffer_store_b128(u32x4{asu(M), tag, asu(l), tag}, rs,
                                             (lane & 46) == 0 ? WS_ML + (h * 16 + s) * 128 + ((lane >> 4) * 2 + (lane & 1)) * 16 : WS_END, 0, SC1);
    }
    u32x4 oA;
    {
#pragma unroll 1
      for (u32 spin = 0; spin < SPIN_CAP * 8; ++spin) {
        if (*(volatile LAS u32*)(L + L_FLAG + 8) == 7u) break;
        __builtin_amdgcn_s_sleep(2);
      }
      asm volatile("" ::: "memory");
      f32x4 t = {0.f, 0.f, 0.f, 0.f};
#pragma unroll
      for (int k = 0; k < 8; ++k) {
        float f0 = *(LAS float*)(L + L_F + k * 8), f1 = *(LAS float*)(L + L_F + k * 8 + 4);
        asm("" : "+v"(f0)); asm("" : "+v"(f1));
        t += *(LAS f32x4*)(L + L_O + k * 1024 + lane * 16) * f32x4{f0, f0, f1, f1};
      }
      oA = opack(t, tag);
      if (!DEFA) __builtin_amdgcn_raw_buffer_store_b128(oA, rs, ooff, 0, SC1);
    }
    const int moff = WS_ML + (hc * 16 + (lane & 15)) * 128 + (rbr * 2 + (rc >> 2)) * 16;
    const int ioff = WS_O + (hc * 16 + rbr * 8 + rc) * 2048 + (lane & 7) * 128 + (lane >> 3) * 16;
    const int br2 = rbr ^ 1;
    const int rr = RED == 0 ? ((j >> 2) * 4 + (x & 3)) * 4 + (j & 3) : j * 4 + (x & 3);
    constexpr int NRED = 72, NY = 4;
    const int yoff = WS_Y + (br2 * 128 + (lane >> 1)) * 2304 + rr * 32 + (lane & 1) * 16;
    const int outoff = (lane & 14) == 0 ? (br2 * 576 + rr * 8 + 4 * (lane & 1) + (lane >> 4)) * 2 : 4096;
    const rsrc_t rout = mkrs(out_, 2304);
    STAMP(7);
    until<slot_ns(35)>(rc ? t0 : t0 - LEAD / 10);
    float w0, w1;
    {
      u32x4 gv;
#pragma unroll 1
      for (u32 spin = 0; spin < SPIN_CAP; ++spin) {
        asm volatile("" ::: "memory");
        gv = ldsc(rs, moff, 0);
        if (none(badtag(gv, tag) != 0)) break;
      }
      const float gm = asf(gv[0]);
      float GM = fmaxf(gm, dpp<XOR1>(gm)); GM = fmaxf(GM, dpp<XOR2>(GM)); GM = fmaxf(GM, dpp<HMIR>(GM)); GM = fmaxf(GM, dpp<ROR8>(GM));
      const float ex = __builtin_amdgcn_exp2f(gm - GM);
      float GL = asf(gv[2]) * ex;
      GL += dpp<XOR1>(GL); GL += dpp<XOR2>(GL); GL += dpp<HMIR>(GL); GL += dpp<ROR8>(GL);
      const float w = ex / GL, wx = dpp<ROR8>(w);
      w0 = (lane & 8) ? wx : w; w1 = (lane & 8) ? w : wx;
    }
    asm volatile("" ::"v"(w0), "v"(w1), "v"(yoff), "v"(ioff), "v"(outoff), "s"(rout));
    STAMP(8);
    if (DEFA) { until<slot_ns(39) + DEFA>(t0); __builtin_amdgcn_raw_buffer_store_b128(oA, rs, ooff, 0, SC1); }
    if (rbr || DEFA != 0) {
#pragma unroll 1
      for (u32 spin = 0; spin < SPIN_CAP * 8; ++spin) {
        if (*(volatile LAS u32*)(L + L_FLAG + 12) == 7u) break;
        __builtin_amdgcn_s_sleep(1);
      }
      until<slot_ns(39) + D1>(t0);
    }
    STAMP(9);
    {
      u32x4 v0, v1;
      u32 spin = 0;
#pragma unroll 1
      for (; spin < SPIN_CAP; ++spin) {
        asm volatile("" ::: "memory");
        v0 = ldsc(rs, ioff, 0) ^ tagb;
        v1 = ldsc(rs, ioff + 1024, 0) ^ tagb;
        if (none(((or4(v0) | or4(v1)) & 0xffu) != 0)) break;
      }
      STAMP(10); STAMPV(20, spin + 1);
      f32x4 ov = asf4(v0) * w0 + asf4(v1) * w1;
      SB();
#pragma unroll
      for (int i = 0; i < 4; ++i) ov[i] += dpp<XOR1>(ov[i]);
      SB();
#pragma unroll
      for (int i = 0; i < 4; ++i) ov[i] += dpp<XOR2>(ov[i]);
      SB();
#pragma unroll
      for (int i = 0; i < 4; ++i) ov[i] += dpp<HMIR>(ov[i]);
      SB();
      const u32x4 e = {cvtpk(ov[0], ov[0]), cvtpk(ov[1], ov[1]), cvtpk(ov[2], ov[2]), cvtpk(ov[3], ov[3])};
      *(LAS u32x4*)(L + L_OF + (lane >> 3) * 16) = e;
    }
    BARRIER();
    STAMP(11);
    if (rr < NRED) {
      until<slot_ns(39) + D1 + D2>(t0);
      f32x4 y;
      u32 spin = 0;
#pragma unroll 1
      for (; spin < SPIN_CAP; ++spin) {
        asm volatile("" ::: "memory");
        u32x4 v[NY];
#pragma unroll
        for (int i = 0; i < NY; ++i) v[i] = ldsc(rs, yoff, i * (128 / NY) * 2304);
        SB();
        u32x4 acc = v[0] ^ tagb;
        y = asf4(acc);
#pragma unroll
        for (int i = 1; i < NY; ++i) { const u32x4 t = v[i] ^ tagb; acc |= t; y += asf4(t); }
        if (none((or4(acc) & 0xffu) != 0)) break;
      }
      STAMP(12); STAMPV(21, spin + 1);
      auto t0_ = __builtin_amdgcn_permlane32_swap(asu(y[0]), asu(y[2]), false, false);
      auto t1_ = __builtin_amdgcn_permlane32_swap(asu(y[1]), asu(y[3]), false, false);
      const float z0 = asf(t0_[0]) + asf(t0_[1]), z1 = asf(t1_[0]) + asf(t1_[1]);
      auto t2_ = __builtin_amdgcn_permlane16_swap(asu(z0), asu(z1), false, false);
      float z = asf(t2_[0]) + asf(t2_[1]);
      z += dpp<ROR8>(z); z += dpp<ROR4>(z);
      if constexpr (RED < 2) z += dpp<XOR2>(z);
      __builtin_amdgcn_raw_buffer_store_b16((unsigned short)cvtpk(z, z), rout, outoff, 0, OAUX);
      if (b == 0) __builtin_amdgcn_raw_buffer_store_b32(tag, rs, lane == 0 ? WS_EPOCH : WS_END, 0, SC1);
    }
    STAMP(13);
    FINISH();
  }
  t0 = __builtin_amdgcn_readfirstlane(*(volatile LAS u32*)(L + L_T0)) + (u32)(a * STAG) / 10u;
  u32x4 msk;
#pragma unroll
  for (int k = 0; k < 4; ++k) msk[k] = ((lane & 7) >> 1) == k ? ((lane & 1) ? 0xffff0000u : 0x0000ffffu) : 0u;
  const int colp = (lane & 7) * 8 + ((lane >> 4) & 1) * 4 + (lane >> 5) * 2;
  auto qproj = [&]() {
    const u32x4 e = {__builtin_amdgcn_perm(xv[0], xv[0], 0x01000100u), __builtin_amdgcn_perm(xv[0], xv[0], 0x03020302u),
                     __builtin_amdgcn_perm(xv[1], xv[1], 0x01000100u), __builtin_amdgcn_perm(xv[1], xv[1], 0x03020302u)};
    const f32x2 qp = gemv_tile(e, msk, W);
    if (!(lane & 8)) *(LAS f32x2*)(L + L_QP + a * 256 + colp * 4) = qp;
  };
  const rsrc_t ro = mkrs((rbr ? woB : woA) + (u32)(hc * 256 + rc * 32) * 1152, 32 * 1152);
  const u32 wooff = (u32)(4 * (lane >> 3)) * 1152 + (lane & 7) * 16 + a * 128;
#define WOS(i) 0
#define WOV(i) ((i) * 1152)
  const int yo = (lane & 8) ? WS_END : WS_Y + (rbr * 128 + hc * 8 + rc) * 2304 + (a * 64 + colp) * 4;
#define ISSUE_WO(i) W[i] = __builtin_amdgcn_raw_buffer_load_b128(ro, wooff + WOV(i), WOS(i), AUXO)
  u32 tag;
  if (a == 8) {
    qproj();
    sfor<0, WOPRE>([&](auto ic) { constexpr int i = decltype(ic)::v; until<slot_ns(40 + i)>(t0); ISSUE_WO(i); });
#pragma unroll 1
    for (u32 spin = 0; spin < SPIN_CAP * 8; ++spin) { if (*(volatile LAS u32*)(L + L_FLAG) == 0) break; __builtin_amdgcn_s_sleep(1); }
    tag = *(volatile LAS u32*)(L + L_FLAG + 4);
  } else {
    const u32 kvo = (u32)(h * 2048 + a * 16 + (s ^ ((a & 1) * 8))) * 1024;
    const u32 koff = (u32)((lane >> 2) & 3) * 256 + (lane & 3) * 64 + (lane >> 4) * 16, voff = lane * 16;
    constexpr u32 KVB = (15u << 17) + 1024;
#define CHUNK(c_) ((c_) < 4 ? (u32)(12 + ((c_) ^ (h & 3))) << 17 : (u32)(((c_) - 4) ^ h) << 17)
    u32x4 K[20], V[20];
    {
      const rsrc_t rkA = mkrs(kA + kvo, KVB), rkB = mkrs(kB + kvo, KVB);
      SB();
      sfor<0, 20>([&](auto cc) {
        constexpr int c = decltype(cc)::v;
        until<slot_ns(c)>(t0);
        VMWAIT(WIN - 1);
        K[c] = ldnt(c < 4 ? rkA : rkB, koff, CHUNK(c));
        SB();
        if constexpr (c == 3) { qproj(); SB(); STAMP(1); }
      });
    }
    const rsrc_t rvA = mkrs(vA + kvo, KVB), rvB = mkrs(vB + kvo, KVB);
    float S[5];
    u32 P[5];
    u32x4 QA, QB, sel = {0u, 0u, 0u, 0u};
    float F0, F1;
    const u32 half = (lane & 1) ? 0xffff0000u : 0x0000ffffu;
    const float LOG2E = 1.4426950408889634f;
    f32x4 o = {0.f, 0.f, 0.f, 0.f};
    LAS unsigned char* op = L + L_O + a * 1024 + lane * 16;
    auto softmax = [&](auto loc, auto hic) {
      constexpr int lo = decltype(loc)::v, hi = decltype(hic)::v;
      float m = S[lo];
#pragma unroll
      for (int g = lo + 1; g < hi; ++g) m = fmaxf(m, S[g]);
      m = allmax4(m);
      float l = 0.f;
#pragma unroll
      for (int g = lo; g < hi; ++g) { const float p = __builtin_amdgcn_exp2f((S[g] - m) * LOG2E); l += p; P[g] = cvtpk(p, p) & half; }
      l = allsum4(l);
      if ((lane & 61) == 0) *(LAS f32x2*)(L + L_ML + (lo ? 128 : 0) + a * 16 + lane * 4) = f32x2{m * LOG2E, l};
    };
    sfor<0, 20 + WIN>([&](auto nc) {
      constexpr int n = decltype(nc)::v, c = n - WIN;
      static_assert(WIN >= QN + 2 && WIN + 4 >= QN + 6, "softmax before P.V");
      if constexpr (c >= 0) {
        sel[0] = bankmov<c & 3, 0>(sel[0], P[c >> 2]); sel[1] = bankmov<c & 3, 1>(sel[1], P[c >> 2]);
        sel[2] = bankmov<c & 3, 2>(sel[2], P[c >> 2]); sel[3] = bankmov<c & 3, 3>(sel[3], P[c >> 2]);
        asm volatile("" : "+v"(sel));
      }
      SB();
      if constexpr (n < 20 + WOPRE) until<slot_ns(20 + n)>(t0);
      VMWAIT(issued_after(n - 1) - (c + 1));
      if constexpr (n < 20) V[n] = ldnt(n < 4 ? rvA : rvB, voff, CHUNK(n));
      else if constexpr (n < 20 + WOPRE) ISSUE_WO(n - 20);
      SB();
      if constexpr (c >= 0) {
        if constexpr (c == 4) o = mfma(sel, V[c], f32x4{0.f, 0.f, 0.f, 0.f});
        else o = mfma(sel, V[c], o);
        asm volatile("" : "+v"(o));
        if constexpr (c == 3) {
          if (DBG == 2) asm volatile("s_nop 7\n\ts_nop 7" : "+v"(o));
          *(LAS f32x4*)op = o;
          if (DBG == 1) asm volatile("s_waitcnt lgkmcnt(0)" ::"v"(o) : "memory");
          asm volatile("s_mov_b64 exec, 1\n\tds_add_u32 %0, %1\n\ts_mov_b64 exec, -1" ::"v"(L + L_FLAG + 8), "v"(1u) : "memory");
        }
      }
      if constexpr (n == QN) {
        STAMP(2);
#pragma unroll 1
        for (u32 spin = 0; spin < SPIN_CAP * 8; ++spin) { if (*(volatile LAS u32*)(L + L_FLAG) == 0) break; __builtin_amdgcn_s_sleep(1); }
        asm volatile("" ::: "memory");
        STAMP(3);
        LAS unsigned char* qp = L + L_Q + ((lane >> 1) & 1) * 256 + ((lane >> 2) & 3) * 64 + (lane >> 4) * 16;
        QA = *(LAS u32x4*)qp;
        QB = *(LAS u32x4*)(qp + 512);
        tag = *(LAS u32*)(L + L_FLAG + 4);
      }
      if constexpr (n >= QN && n < QN + 5) {
        constexpr int g = n - QN;
        u32 T[4] = {0u, 0u, 0u, 0u};
        sfor<0, 4>([&](auto ic) {
          constexpr int i = decltype(ic)::v;
          const f32x4 d = mfma(K[g * 4 + i], g ? QB : QA, f32x4{0.f, 0.f, 0.f, 0.f});
          T[0] = bankmov<0, i>(T[0], asu(d[0])); T[1] = bankmov<1, i>(T[1], asu(d[1]));
          T[2] = bankmov<2, i>(T[2], asu(d[2])); T[3] = bankmov<3, i>(T[3], asu(d[3]));
        });
        S[g] = rbf(rbf((asf(T[0]) + asf(T[1])) + (asf(T[2]) + asf(T[3]))) * 0.08838834764831845f);
      }
      if constexpr (n == QN + 1) softmax(IC<0>{}, IC<1>{});
      if constexpr (n == QN + 5) { softmax(IC<1>{}, IC<5>{}); STAMP(4); }
      if constexpr (n == 18) {
#pragma unroll 1
        for (u32 spin = 0; spin < SPIN_CAP * 8; ++spin) {
          const u32x2 f = *(volatile LAS u32x2*)(L + L_F + 64 + a * 8);
          F0 = asf(f[0]); F1 = asf(f[1]);
          if (max(f[0], f[1]) != SENT) break;
          __builtin_amdgcn_s_sleep(1);
        }
        asm volatile("" : "+v"(F0), "+v"(F1) : "v"(yo), "v"(ooff), "v"(msk), "v"(wooff));
      }
      SB();
    });
    *(LAS f32x4*)(op + 8192) = o * f32x4{F0, F0, F1, F1};
    u32 arr;
    asm volatile("s_mov_b64 exec, 1\n\tds_add_rtn_u32 %0, %1, %2\n\ts_mov_b64 exec, -1\n\ts_waitcnt lgkmcnt(0)"
                 : "=&v"(arr) : "v"(L + L_FLAG + 12), "v"(1u) : "memory");
    arr = __builtin_amdgcn_readfirstlane(arr);
    STAMP(5);
    if (arr == 6u) {
      f32x4 u[8];
#pragma unroll
      for (int k = 0; k < 8; ++k) u[k] = *(LAS f32x4*)(L + L_O + 8192 + k * 1024 + lane * 16);
      const f32x4 t = ((u[0] + u[1]) + (u[2] + u[3])) + ((u[4] + u[5]) + (u[6] + u[7]));
      __builtin_amdgcn_raw_buffer_store_b128(opack(t, tag), rs, ooff, 8 * 2048, SC1);
      STAMP(8);
    }
  }
  sfor<WOPRE, 4>([&](auto ic) {
    constexpr int i = decltype(ic)::v;
    until<slot_ns(40 + i)>(t0);
    if constexpr (i == 3 && W3TH >= 0) until<slot_ns(39) + D1 + 30>(__builtin_amdgcn_readfirstlane(*(volatile LAS u32*)(L + L_T0 + 4)));
    ISSUE_WO(i);
  });
  SB();
  BARRIER();
  const u32x4 e = *(LAS u32x4*)(L + L_OF + (lane >> 3) * 16);
  const f32x2 y = gemv_tile(e, msk, W);
  __builtin_amdgcn_raw_buffer_store_b64(u32x2{tw(asu(y[0]), tag), tw(asu(y[1]), tag)}, rs, yo, 0, SC1);
  STAMP(7);
  FINISH();
}

extern "C" void kernel_launch(void* const* d_in, const int* in_sizes, int n_in, void* d_out, int out_size, void* d_ws,
                              size_t ws_size, hipStream_t stream) {
  typedef const unsigned char* P;
  fused_decode<<<256, NTHR, 0, stream>>>((P)d_in[0], (P)d_in[1], (P)d_in[2], (P)d_in[3], (P)d_in[4], (P)d_in[5], (P)d_in[6],
                                         (P)d_in[7], (P)d_in[8], (P)d_in[9], (unsigned char*)d_out, (unsigned char*)d_ws);
}
```
